```python
import torch

def _softmax(x, axis=-1):
    return torch.softmax(x, dim=axis)

def _gelu(x):
    return torch.nn.functional.gelu(x, approximate='tanh')

def _silu(x):
    return torch.nn.functional.silu(x)

def _sigmoid(x):
    return torch.sigmoid(x)

def _layernorm(x, axis=-1, eps=1e-05):
    m = x.mean(dim=axis, keepdim=True)
    v = x.var(dim=axis, keepdim=True, unbiased=False)
    return (x - m) / torch.sqrt(v + eps)

def _scatter_add(tgt, idx, src, axis=0):
    return tgt.clone().index_add_(axis, idx, src)

def _segment_sum(src, seg, n):
    out = torch.zeros((n,) + src.shape[1:], dtype=src.dtype)
    return out.index_add_(0, seg, src)

def _pad(x, pw):
    flat = tuple((v for pair in reversed(pw) for v in pair))
    return torch.nn.functional.pad(x, flat)
kernel_name = 'concat_heads__sliding_block__memory_aug_recurrent_mem_tr__prompt'

def setup_inputs():

    _w = lambda a: (lambda t: t.to(torch.bfloat16) if t.is_floating_point() else t)(torch.as_tensor(a))

    def _A_setup():
        B, S, D = (1, 2048, 1024)
        return {'x': _w((lambda *s: torch.randn(*s, dtype=torch.bfloat16) * 0.2)(B, S, D)), 'wqkv': _w((lambda *s: torch.randn(*s, dtype=torch.bfloat16) * 0.2)(D, 3 * D)), 'wo': _w((lambda *s: torch.randn(*s, dtype=torch.bfloat16) * 0.05)(D, D))}
    _A_in = _A_setup()

    def _B_setup():
        B, S, Dm = (1, 2048, 1024)
        H, Dh = (32, 128)
        M, seg = (8, 32)
        seg = min(seg, S)
        S = S // seg * seg
        return {'x': _w((lambda *s: torch.randn(*s, dtype=torch.bfloat16) * 0.05)(B, S, Dm)), 'mem0': _w((lambda *s: torch.randn(*s, dtype=torch.bfloat16) * 1.6)(B, M, Dm)), 'Wq': _w((lambda *s: torch.randn(*s, dtype=torch.bfloat16) * 0.8)(Dm, H, Dh) * Dm ** (-0.5)), 'Wk': _w((lambda *s: torch.randn(*s, dtype=torch.bfloat16) * 0.8)(Dm, H, Dh) * Dm ** (-0.5)), 'Wv': _w((lambda *s: torch.randn(*s, dtype=torch.bfloat16) * 1.6)(Dm, H, Dh) * Dm ** (-0.5)), 'Wo': _w((lambda *s: torch.randn(*s, dtype=torch.bfloat16) * 1.6)(H, Dh, Dm) * (H * Dh) ** (-0.5)), 'seg_len': seg}
    _B_in = _B_setup()
    return {'A_x': _A_in['x'], 'A_wqkv': _A_in['wqkv'], 'A_wo': _A_in['wo'], 'B_x': _B_in['x'], 'B_mem0': _B_in['mem0'], 'B_Wq': _B_in['Wq'], 'B_Wk': _B_in['Wk'], 'B_Wv': _B_in['Wv'], 'B_Wo': _B_in['Wo'], 'B_seg_len': _B_in['seg_len']}

def reference(A_x, A_wqkv, A_wo, B_x, B_mem0, B_Wq, B_Wk, B_Wv, B_Wo, B_seg_len):

    def _A_ref(x, wqkv, wo):
        B, S, D = x.shape
        H = 32
        Dh = D // H
        W = 256
        qkv = (lambda x, *d: x.permute(*d))((x @ wqkv).reshape(B, S, 3, H, Dh), 2, 0, 3, 1, 4)
        q, k, v = (qkv[0], qkv[1], qkv[2])
        scores = torch.einsum('bhqd,bhkd->bhqk', q, k) * Dh ** (-0.5)
        idx = torch.arange(S)
        mask = (idx[:, None] - idx[None, :] >= 0) & (idx[:, None] - idx[None, :] < W)
        scores = torch.where(mask[None, None], scores, -1000000000.0)
        p = _softmax(scores, axis=-1)
        o = torch.einsum('bhqk,bhkd->bhqd', p, v)
        return (lambda x, *d: x.permute(*d))(o, 0, 2, 1, 3).reshape(B, S, D) @ wo

    def _B_ref(x, mem0, Wq, Wk, Wv, Wo, seg_len):
        B, S, Dm = x.shape
        H, Dh = (Wq.shape[1], Wq.shape[2])
        M = mem0.shape[1]
        scale = Dh ** (-0.5)
        n_seg = S // seg_len
        L = M + seg_len
        idx = torch.arange(L)
        mask = (idx[:, None] < M) | (idx[:, None] >= idx[None, :])
        mem = mem0
        outs = []
        for s in range(n_seg):
            xs = x[:, s * seg_len:(s + 1) * seg_len, :].clone()
            h = torch.concatenate([mem, xs], axis=1)
            q = torch.einsum('btd,dhe->bhte', h, Wq)
            k = torch.einsum('btd,dhe->bhte', h, Wk)
            v = torch.einsum('btd,dhe->bhte', h, Wv)
            scores = torch.einsum('bhqe,bhke->bhqk', q, k) * scale
            scores = torch.where(mask[None, None], scores, -1000000000.0)
            p = _softmax(scores, axis=-1)
            o = torch.einsum('bhqk,bhke->bhqe', p, v)
            o = torch.einsum('bhte,hed->btd', o, Wo)
            mem = o[:, :M, :].clone()
            outs.append(o[:, M:, :])
        return torch.concatenate(outs, axis=1)
    a = _A_ref(A_x, A_wqkv, A_wo)
    b = _B_ref(B_x, B_mem0, B_Wq, B_Wk, B_Wv, B_Wo, B_seg_len)
    return torch.concatenate([a, b], axis=-1)
```

```cpp
#include <hip/hip_runtime.h>
#include <hip/hip_bf16.h>
#include <stdint.h>

constexpr int S_LEN = 2048;
constexpr int D_MODEL = 1024;
constexpr int A_H = 32, A_DH = 32, A_W = 256;
constexpr int B_H = 32, B_DH = 128, B_M = 8, B_SEG = 32, B_NSEG = 64;
constexpr int B_HD = B_H * B_DH;
constexpr int QKVB_LD = 3 * B_HD;
constexpr int QKVA_LD = 3 * D_MODEL;
constexpr int OUT_LD = 2 * D_MODEL;

typedef unsigned short u16;
typedef __attribute__((ext_vector_type(8))) short bf16x8;
typedef __attribute__((ext_vector_type(4))) short bf16x4;
typedef __attribute__((ext_vector_type(4))) float f32x4;
typedef __attribute__((ext_vector_type(4))) unsigned u32x4;
#define GAS __attribute__((address_space(1)))
typedef GAS unsigned gu32;
typedef GAS unsigned long long gu64;
#define RLX_AGENT __ATOMIC_RELAXED, __HIP_MEMORY_SCOPE_AGENT

__device__ __forceinline__ float bf2f(u16 v) { return __uint_as_float(((unsigned)v) << 16); }
__device__ __forceinline__ u16 f2bf(float f) {
  __hip_bfloat16 b = __float2bfloat16(f);
  return *reinterpret_cast<u16*>(&b);
}
__device__ __forceinline__ float rbf(float f) { return bf2f(f2bf(f)); }

__device__ __forceinline__ unsigned boff(unsigned row, unsigned ch) {
  return 256u * row + 16u * (ch ^ (((row & 3) << 2) | ((row >> 2) & 3)));
}

__global__ __launch_bounds__(256, 2) void gemm128(const u16* __restrict__ A, int lda,
                                                  const u16* __restrict__ B, int ldb,
                                                  u16* __restrict__ C, int ldc, int M, int N, int K) {
  __shared__ __attribute__((aligned(16))) char smem[2 * (8192 + 8192)];
  const int nbn = N / 128;
  const int bm = blockIdx.x / nbn, bn = blockIdx.x % nbn;
  const int tid = threadIdx.x, lane = tid & 63, wid = tid >> 6;
  const int wr = wid >> 1, wc = wid & 1;
  const int row0 = bm * 128, col0 = bn * 128;
  u32x4 ra[2], rb[2];
  auto gload = [&](int kt) {
#pragma unroll
    for (int i = 0; i < 2; ++i) {
      int c = tid + 256 * i;
      int r = c >> 2, ch = c & 3;
      ra[i] = *reinterpret_cast<const u32x4*>(A + (size_t)(row0 + r) * lda + kt * 32 + ch * 8);
      int kr = c >> 4, chb = c & 15;
      rb[i] = *reinterpret_cast<const u32x4*>(B + (size_t)(kt * 32 + kr) * ldb + col0 + chb * 8);
    }
  };
  auto swrite = [&](int buf) {
    char* sa = smem + buf * 16384;
    char* sb = sa + 8192;
#pragma unroll
    for (int i = 0; i < 2; ++i) {
      int c = tid + 256 * i;
      int r = c >> 2, ch = c & 3;
      *reinterpret_cast<u32x4*>(sa + r * 64 + ch * 16) = ra[i];
      int kr = c >> 4, chb = c & 15;
      *reinterpret_cast<u32x4*>(sb + boff(kr, chb)) = rb[i];
    }
  };
  f32x4 acc[4][4];
#pragma unroll
  for (int m = 0; m < 4; ++m)
#pragma unroll
    for (int n = 0; n < 4; ++n) acc[m][n] = (f32x4){0.f, 0.f, 0.f, 0.f};
  const int nk = K / 32;
  gload(0);
  swrite(0);
  __syncthreads();
  for (int kt = 0; kt < nk; ++kt) {
    const int buf = kt & 1;
    if (kt + 1 < nk) gload(kt + 1);
    const char* sa = smem + buf * 16384;
    const char* sb = sa + 8192;
    bf16x8 af[4], bfr[4];
#pragma unroll
    for (int m = 0; m < 4; ++m)
      af[m] = *reinterpret_cast<const bf16x8*>(sa + (wr * 64 + m * 16 + (lane & 15)) * 64 + (lane >> 4) * 16);
    {
      const unsigned g = lane >> 4, q = (lane & 15) >> 2, p = lane & 3;
      const unsigned base = (unsigned)(size_t)sb;
#pragma unroll
      for (int n = 0; n < 4; ++n) {
        const unsigned c = wc * 4 + n;
        unsigned a0 = base + boff(8 * g + 0 + q, 2 * c + (p >> 1)) + 8 * (p & 1);
        unsigned a1 = base + boff(8 * g + 4 + q, 2 * c + (p >> 1)) + 8 * (p & 1);
        bf16x4 lo, hi;
        asm volatile("ds_read_b64_tr_b16 %0, %2\n\tds_read_b64_tr_b16 %1, %3\n\ts_waitcnt lgkmcnt(0)"
                     : "=&v"(lo), "=&v"(hi)
                     : "v"(a0), "v"(a1)
                     : "memory");
        bfr[n] = (bf16x8){lo[0], lo[1], lo[2], lo[3], hi[0], hi[1], hi[2], hi[3]};
      }
    }
#pragma unroll
    for (int m = 0; m < 4; ++m)
#pragma unroll
      for (int n = 0; n < 4; ++n)
        acc[m][n] = __builtin_amdgcn_mfma_f32_16x16x32_bf16(af[m], bfr[n], acc[m][n], 0, 0, 0);
    if (kt + 1 < nk) swrite(buf ^ 1);
    __syncthreads();
  }
#pragma unroll
  for (int m = 0; m < 4; ++m)
#pragma unroll
    for (int n = 0; n < 4; ++n)
#pragma unroll
      for (int j = 0; j < 4; ++j)
        C[(size_t)(row0 + wr * 64 + m * 16 + (lane >> 4) * 4 + j) * ldc + col0 + wc * 64 + n * 16 + (lane & 15)] =
            f2bf(acc[m][n][j]);
}

__global__ __launch_bounds__(256, 2) void attnA(const u16* __restrict__ qkv, u16* __restrict__ o) {
  const int h = blockIdx.x % A_H, qb = blockIdx.x / A_H;
  const int tid = threadIdx.x, lane = tid & 63, wid = tid >> 6;
  const int q0 = qb * 64;
  const int kbase = q0 - 256;
  __shared__ __attribute__((aligned(16))) u16 sK[320 * 32];
  __shared__ __attribute__((aligned(16))) u16 sV[320 * 32];
  __shared__ __attribute__((aligned(16))) u16 sP[4][16 * 296];
  for (int c = tid; c < 320 * 4; c += 256) {
    int r = c >> 2, ch = c & 3;
    int key = kbase + r;
    u32x4 kv = (u32x4){0, 0, 0, 0}, vv = (u32x4){0, 0, 0, 0};
    if (key >= 0) {
      kv = *reinterpret_cast<const u32x4*>(qkv + (size_t)key * QKVA_LD + D_MODEL + h * 32 + ch * 8);
      vv = *reinterpret_cast<const u32x4*>(qkv + (size_t)key * QKVA_LD + 2 * D_MODEL + h * 32 + ch * 8);
    }
    *reinterpret_cast<u32x4*>(sK + r * 32 + ch * 8) = kv;
    *reinterpret_cast<u32x4*>(sV + r * 32 + ch * 8) = vv;
  }
  __syncthreads();
  const int qr0 = q0 + wid * 16;
  bf16x8 qf = *reinterpret_cast<const bf16x8*>(qkv + (size_t)(qr0 + (lane & 15)) * QKVA_LD + h * 32 + (lane >> 4) * 8);
  const int t0 = wid;
  f32x4 sc[17];
  const float scale = 0.17677669529663687f;
  float mx[4] = {-3e38f, -3e38f, -3e38f, -3e38f};
#pragma unroll
  for (int t = 0; t < 17; ++t) {
    bf16x8 kf = *reinterpret_cast<const bf16x8*>(sK + ((t0 + t) * 16 + (lane & 15)) * 32 + (lane >> 4) * 8);
    f32x4 s = __builtin_amdgcn_mfma_f32_16x16x32_bf16(qf, kf, (f32x4){0.f, 0.f, 0.f, 0.f}, 0, 0, 0);
    const int key = kbase + (t0 + t) * 16 + (lane & 15);
#pragma unroll
    for (int j = 0; j < 4; ++j) {
      const int row = qr0 + (lane >> 4) * 4 + j;
      float v = rbf(rbf(s[j]) * scale);
      const bool ok = (key <= row) && (row - key < A_W) && (key >= 0);
      v = ok ? v : -1e9f;
      s[j] = v;
      mx[j] = fmaxf(mx[j], v);
    }
    sc[t] = s;
  }
#pragma unroll
  for (int j = 0; j < 4; ++j) {
#pragma unroll
    for (int m = 1; m < 16; m <<= 1) mx[j] = fmaxf(mx[j], __shfl_xor(mx[j], m));
  }
  float sum[4] = {0.f, 0.f, 0.f, 0.f};
#pragma unroll
  for (int t = 0; t < 17; ++t)
#pragma unroll
    for (int j = 0; j < 4; ++j) {
      float e = __expf(sc[t][j] - mx[j]);
      sc[t][j] = e;
      sum[j] += e;
    }
#pragma unroll
  for (int j = 0; j < 4; ++j) {
#pragma unroll
    for (int m = 1; m < 16; m <<= 1) sum[j] += __shfl_xor(sum[j], m);
    sum[j] = 1.f / sum[j];
  }
  u16* P = sP[wid];
#pragma unroll
  for (int t = 0; t < 17; ++t)
#pragma unroll
    for (int j = 0; j < 4; ++j) P[((lane >> 4) * 4 + j) * 296 + t * 16 + (lane & 15)] = f2bf(sc[t][j] * sum[j]);
#pragma unroll
  for (int j = 0; j < 4; ++j) P[((lane >> 4) * 4 + j) * 296 + 272 + (lane & 15)] = 0;
  __syncthreads();
  f32x4 oacc[2] = {(f32x4){0.f, 0.f, 0.f, 0.f}, (f32x4){0.f, 0.f, 0.f, 0.f}};
  for (int ks = 0; ks < 9; ++ks) {
    bf16x8 pf = *reinterpret_cast<const bf16x8*>(P + (lane & 15) * 296 + ks * 32 + (lane >> 4) * 8);
#pragma unroll
    for (int n = 0; n < 2; ++n) {
      bf16x8 vf;
#pragma unroll
      for (int j = 0; j < 8; ++j) {
        int kl = t0 * 16 + ks * 32 + (lane >> 4) * 8 + j;
        kl = kl < 320 ? kl : 319;
        vf[j] = (short)sV[kl * 32 + n * 16 + (lane & 15)];
      }
      oacc[n] = __builtin_amdgcn_mfma_f32_16x16x32_bf16(pf, vf, oacc[n], 0, 0, 0);
    }
  }
#pragma unroll
  for (int n = 0; n < 2; ++n)
#pragma unroll
    for (int j = 0; j < 4; ++j)
      o[(size_t)(qr0 + (lane >> 4) * 4 + j) * D_MODEL + h * 32 + n * 16 + (lane & 15)] = f2bf(oacc[n][j]);
}

__global__ __launch_bounds__(256, 2) void attnBx(const u16* __restrict__ qkv, const u16* __restrict__ kvm,
                                                 u16* __restrict__ o) {
  const int seg = blockIdx.x / 8, hg = blockIdx.x % 8;
  const int tid = threadIdx.x, lane = tid & 63, wid = tid >> 6;
  const int h = hg * 4 + wid;
  __shared__ __attribute__((aligned(16))) u16 sK[4][48 * 136];
  __shared__ __attribute__((aligned(16))) u16 sV[4][64 * 136];
  __shared__ __attribute__((aligned(16))) u16 sP[4][32 * 72];
  u16* K = sK[wid];
  u16* V = sV[wid];
  u16* P = sP[wid];
  for (int c = lane; c < 64 * 16; c += 64) {
    int r = c >> 4, ch = c & 15;
    u32x4 kv = (u32x4){0, 0, 0, 0}, vv = (u32x4){0, 0, 0, 0};
    if (r < 8) {
      kv = *reinterpret_cast<const u32x4*>(kvm + ((size_t)(seg * 2 + 0) * 8 + r) * B_HD + h * 128 + ch * 8);
      vv = *reinterpret_cast<const u32x4*>(kvm + ((size_t)(seg * 2 + 1) * 8 + r) * B_HD + h * 128 + ch * 8);
    } else if (r < 40) {
      const u16* rowp = qkv + (size_t)(seg * 32 + r - 8) * QKVB_LD;
      kv = *reinterpret_cast<const u32x4*>(rowp + B_HD + h * 128 + ch * 8);
      vv = *reinterpret_cast<const u32x4*>(rowp + 2 * B_HD + h * 128 + ch * 8);
    }
    if (r < 48) *reinterpret_cast<u32x4*>(K + r * 136 + ch * 8) = kv;
    *reinterpret_cast<u32x4*>(V + r * 136 + ch * 8) = vv;
  }
  __syncthreads();
  const float scale = 0.08838834764831845f;
#pragma unroll
  for (int mt = 0; mt < 2; ++mt) {
    bf16x8 qf[4];
#pragma unroll
    for (int ks = 0; ks < 4; ++ks)
      qf[ks] = *reinterpret_cast<const bf16x8*>(qkv + (size_t)(seg * 32 + mt * 16 + (lane & 15)) * QKVB_LD + h * 128 +
                                                ks * 32 + (lane >> 4) * 8);
    f32x4 sc[3];
    float mx[4] = {-3e38f, -3e38f, -3e38f, -3e38f};
#pragma unroll
    for (int t = 0; t < 3; ++t) {
      f32x4 s = (f32x4){0.f, 0.f, 0.f, 0.f};
#pragma unroll
      for (int ks = 0; ks < 4; ++ks) {
        bf16x8 kf = *reinterpret_cast<const bf16x8*>(K + (t * 16 + (lane & 15)) * 136 + ks * 32 + (lane >> 4) * 8);
        s = __builtin_amdgcn_mfma_f32_16x16x32_bf16(qf[ks], kf, s, 0, 0, 0);
      }
      const int key = t * 16 + (lane & 15);
#pragma unroll
      for (int j = 0; j < 4; ++j) {
        const int rowp = 8 + mt * 16 + (lane >> 4) * 4 + j;
        float v = rbf(rbf(s[j]) * scale);
        const bool ok = (key <= rowp);
        v = ok ? v : -1e9f;
        s[j] = v;
        mx[j] = fmaxf(mx[j], v);
      }
      sc[t] = s;
    }
#pragma unroll
    for (int j = 0; j < 4; ++j) {
#pragma unroll
      for (int m = 1; m < 16; m <<= 1) mx[j] = fmaxf(mx[j], __shfl_xor(mx[j], m));
    }
    float sum[4] = {0.f, 0.f, 0.f, 0.f};
#pragma unroll
    for (int t = 0; t < 3; ++t)
#pragma unroll
      for (int j = 0; j < 4; ++j) {
        float e = __expf(sc[t][j] - mx[j]);
        sc[t][j] = e;
        sum[j] += e;
      }
#pragma unroll
    for (int j = 0; j < 4; ++j) {
#pragma unroll
      for (int m = 1; m < 16; m <<= 1) sum[j] += __shfl_xor(sum[j], m);
      sum[j] = 1.f / sum[j];
    }
#pragma unroll
    for (int t = 0; t < 3; ++t)
#pragma unroll
      for (int j = 0; j < 4; ++j)
        P[(mt * 16 + (lane >> 4) * 4 + j) * 72 + t * 16 + (lane & 15)] = f2bf(sc[t][j] * sum[j]);
#pragma unroll
    for (int j = 0; j < 4; ++j) P[(mt * 16 + (lane >> 4) * 4 + j) * 72 + 48 + (lane & 15)] = 0;
  }
  __syncthreads();
#pragma unroll
  for (int mt = 0; mt < 2; ++mt) {
    bf16x8 pf[2];
#pragma unroll
    for (int ks = 0; ks < 2; ++ks)
      pf[ks] = *reinterpret_cast<const bf16x8*>(P + (mt * 16 + (lane & 15)) * 72 + ks * 32 + (lane >> 4) * 8);
#pragma unroll
    for (int n = 0; n < 8; ++n) {
      f32x4 oacc = (f32x4){0.f, 0.f, 0.f, 0.f};
#pragma unroll
      for (int ks = 0; ks < 2; ++ks) {
        bf16x8 vf;
#pragma unroll
        for (int j = 0; j < 8; ++j) vf[j] = (short)V[(ks * 32 + (lane >> 4) * 8 + j) * 136 + n * 16 + (lane & 15)];
        oacc = __builtin_amdgcn_mfma_f32_16x16x32_bf16(pf[ks], vf, oacc, 0, 0, 0);
      }
#pragma unroll
      for (int j = 0; j < 4; ++j)
        o[(size_t)(seg * 32 + mt * 16 + (lane >> 4) * 4 + j) * B_HD + h * 128 + n * 16 + (lane & 15)] = f2bf(oacc[j]);
    }
  }
}

constexpr unsigned kSpinCap = 1u << 22;

__device__ __forceinline__ bool spin_fail(unsigned& spins, gu32* tmo, int lane) {
  __builtin_amdgcn_s_sleep(1);
  if (++spins > kSpinCap) {
    if (lane == 0) __hip_atomic_store(tmo, 1u, RLX_AGENT);
    return true;
  }
  return (spins & 255u) == 0u && __builtin_amdgcn_readfirstlane(__hip_atomic_load(tmo, RLX_AGENT));
}
__device__ __forceinline__ void store_granule(gu64* g, unsigned epoch, unsigned value) {
  __hip_atomic_store(g, ((unsigned long long)epoch << 32) | value, RLX_AGENT);
}
template <int N>
__device__ __forceinline__ bool sweep_granules(gu64* g, int S, unsigned epoch, unsigned (&v)[N], gu32* tmo, int lane) {
  for (unsigned spins = 0;;) {
    bool ok = true;
#pragma unroll
    for (int k = 0; k < N; ++k) {
      const unsigned long long x = __hip_atomic_load(g + k * S, RLX_AGENT);
      v[k] = (unsigned)x;
      ok &= (x >> 32) == epoch;
    }
    if (__all(ok)) return true;
    if (spin_fail(spins, tmo, lane)) return false;
  }
}

struct ChainArgs {
  const u16* mem0;
  const u16* Wq;
  const u16* Wk;
  const u16* Wv;
  const u16* Wo;
  const u16* qkvB;
  u16* kvm;
  unsigned long long* gMem;
  unsigned long long* gQK;
  unsigned long long* gO;
  unsigned* tmo;
};

constexpr int MEM_LDS_STRIDE = 1032;

__global__ __launch_bounds__(512, 2) void chain_kernel(ChainArgs a) {
  extern __shared__ __attribute__((aligned(16))) char lds[];
  const int b = blockIdx.x;
  const int tid = threadIdx.x, lane = tid & 63;
  const int w = __builtin_amdgcn_readfirstlane(tid >> 6);
  gu64* gMem = (gu64*)a.gMem;
  gu64* gQK = (gu64*)a.gQK;
  gu64* gO = (gu64*)a.gO;
  gu32* tmo = (gu32*)a.tmo;

  if (b < 192) {
    const int t = b / 64, cb = b % 64;
    const u16* W = t == 0 ? a.Wq : (t == 1 ? a.Wk : a.Wv);
    const int nt = w & 3, kq = w >> 2;
    const int col = cb * 64 + nt * 16 + (lane & 15);
    bf16x8 wf[16];
#pragma unroll
    for (int i = 0; i < 16; ++i) {
#pragma unroll
      for (int j = 0; j < 8; ++j)
        wf[i][j] = (short)W[(size_t)(kq * 512 + i * 32 + (lane >> 4) * 8 + j) * B_HD + col];
      asm volatile("" : "+v"(wf[i]));
    }
    u16* sMem = (u16*)lds;
    float* sRed = (float*)(lds + 33280);
    u16* sOut = (u16*)(lds + 33280 + 8192);
    float* sQ = (float*)(lds + 33280 + 8192 + 1024);
    float* sKm = sQ + 8 * 128;
    u16* sKx = (u16*)(sKm + 8 * 128);
    u16* sVx = sKx + 32 * 136;
    float* sS = (float*)(sVx + 32 * 72);
    for (int i = tid; i < 8 * MEM_LDS_STRIDE; i += 512) sMem[8 * MEM_LDS_STRIDE + i] = 0;
    const int h = cb >> 1, half = cb & 1;
    for (int s = 0; s < B_NSEG; ++s) {
      const unsigned epoch = (unsigned)s + 1u;
      u32x4 kxr = (u32x4){0, 0, 0, 0}, vxr = (u32x4){0, 0, 0, 0};
      if (t == 2) {
        {
          int r = tid >> 4, ch = tid & 15;
          kxr = *reinterpret_cast<const u32x4*>(a.qkvB + (size_t)(s * 32 + r) * QKVB_LD + B_HD + h * 128 + ch * 8);
        }
        if (tid < 256) {
          int r = tid >> 3, ch = tid & 7;
          vxr = *reinterpret_cast<const u32x4*>(a.qkvB + (size_t)(s * 32 + r) * QKVB_LD + 2 * B_HD + h * 128 +
                                                half * 64 + ch * 8);
        }
      }
      if (s == 0) {
#pragma unroll
        for (int k = 0; k < 8; ++k) {
          int g = tid + 512 * k;
          int r = g >> 9, cp = g & 511;
          unsigned v = *reinterpret_cast<const unsigned*>(a.mem0 + r * 1024 + cp * 2);
          *reinterpret_cast<unsigned*>(sMem + r * MEM_LDS_STRIDE + cp * 2) = v;
        }
      } else {
        unsigned v[8];
        if (!sweep_granules<8>(gMem + tid, 512, (unsigned)s, v, tmo, lane)) return;
#pragma unroll
        for (int k = 0; k < 8; ++k) {
          int g = tid + 512 * k;
          int r = g >> 9, cp = g & 511;
          *reinterpret_cast<unsigned*>(sMem + r * MEM_LDS_STRIDE + cp * 2) = v[k];
        }
      }
      __syncthreads();
      f32x4 acc = (f32x4){0.f, 0.f, 0.f, 0.f};
#pragma unroll
      for (int i = 0; i < 16; ++i) {
        bf16x8 af = *reinterpret_cast<const bf16x8*>(sMem + (lane & 15) * MEM_LDS_STRIDE + kq * 512 + i * 32 +
                                                     (lane >> 4) * 8);
        acc = __builtin_amdgcn_mfma_f32_16x16x32_bf16(af, wf[i], acc, 0, 0, 0);
      }
      if (lane < 32) {
#pragma unroll
        for (int j = 0; j < 4; ++j) sRed[(kq * 8 + (lane >> 4) * 4 + j) * 64 + nt * 16 + (lane & 15)] = acc[j];
      }
      __syncthreads();
      if (tid < 256) {
        int r = tid >> 5, cp = tid & 31;
        float v0 = sRed[r * 64 + cp * 2] + sRed[(8 + r) * 64 + cp * 2];
        float v1 = sRed[r * 64 + cp * 2 + 1] + sRed[(8 + r) * 64 + cp * 2 + 1];
        unsigned pk = (unsigned)f2bf(v0) | ((unsigned)f2bf(v1) << 16);
        if (t < 2) {
          store_granule(gQK + (size_t)t * 16384 + r * 2048 + cb * 32 + cp, epoch, pk);
        }
        if (t >= 1) {
          *reinterpret_cast<unsigned*>(a.kvm + ((size_t)(s * 2 + (t - 1)) * 8 + r) * B_HD + cb * 64 + cp * 2) = pk;
        }
        if (t == 2) *reinterpret_cast<unsigned*>(sOut + r * 64 + cp * 2) = pk;
      }
      if (t == 2) {
        {
          int r = tid >> 4, ch = tid & 15;
          *reinterpret_cast<u32x4*>(sKx + r * 136 + ch * 8) = kxr;
        }
        if (tid < 256) {
          int r = tid >> 3, ch = tid & 7;
          *reinterpret_cast<u32x4*>(sVx + r * 72 + ch * 8) = vxr;
        }
        {
          unsigned v[2];
          int r = tid >> 6, cp = tid & 63;
          if (!sweep_granules<2>(gQK + r * 2048 + h * 64 + cp, 16384, epoch, v, tmo, lane)) return;
          sQ[r * 128 + cp * 2] = bf2f((u16)(v[0] & 0xffff));
          sQ[r * 128 + cp * 2 + 1] = bf2f((u16)(v[0] >> 16));
          sKm[r * 128 + cp * 2] = bf2f((u16)(v[1] & 0xffff));
          sKm[r * 128 + cp * 2 + 1] = bf2f((u16)(v[1] >> 16));
        }
        __syncthreads();
        if (tid < 320) {
          int r = tid / 40, key = tid % 40;
          float d = 0.f;
          if (key < 8) {
            for (int e = 0; e < 128; ++e) d += sQ[r * 128 + e] * sKm[key * 128 + e];
          } else {
            for (int e = 0; e < 128; ++e) d += sQ[r * 128 + e] * bf2f(sKx[(key - 8) * 136 + e]);
          }
          sS[r * 40 + key] = rbf(rbf(d) * 0.08838834764831845f);
        }
        __syncthreads();
        if (tid < 8) {
          float mx = -3e38f;
          for (int k = 0; k < 40; ++k) mx = fmaxf(mx, sS[tid * 40 + k]);
          float sum = 0.f;
          for (int k = 0; k < 40; ++k) {
            float e = __expf(sS[tid * 40 + k] - mx);
            sS[tid * 40 + k] = e;
            sum += e;
          }
          float inv = 1.f / sum;
          for (int k = 0; k < 40; ++k) sS[tid * 40 + k] = rbf(sS[tid * 40 + k] * inv);
        }
        __syncthreads();
        if (tid < 256) {
          int r = tid >> 5, cp = tid & 31;
          float o0 = 0.f, o1 = 0.f;
          for (int k = 0; k < 8; ++k) {
            float p = sS[r * 40 + k];
            o0 += p * bf2f(sOut[k * 64 + cp * 2]);
            o1 += p * bf2f(sOut[k * 64 + cp * 2 + 1]);
          }
          for (int k = 0; k < 32; ++k) {
            float p = sS[r * 40 + 8 + k];
            o0 += p * bf2f(sVx[k * 72 + cp * 2]);
            o1 += p * bf2f(sVx[k * 72 + cp * 2 + 1]);
          }
          unsigned pk = (unsigned)f2bf(o0) | ((unsigned)f2bf(o1) << 16);
          store_granule(gO + r * 2048 + cb * 32 + cp, epoch, pk);
        }
      }
      __syncthreads();
    }
  } else {
    const int ob = b - 192;
    const int col = ob * 16 + (lane & 15);
    bf16x8 wf[16];
#pragma unroll
    for (int i = 0; i < 16; ++i) {
#pragma unroll
      for (int j = 0; j < 8; ++j)
        wf[i][j] = (short)a.Wo[(size_t)(w * 512 + i * 32 + (lane >> 4) * 8 + j) * D_MODEL + col];
      asm volatile("" : "+v"(wf[i]));
    }
    u16* sO = (u16*)lds + w * (8 * 520);
    float* sRed = (float*)(lds + 8 * 8 * 520 * 2);
    for (int s = 0; s < B_NSEG; ++s) {
      const unsigned epoch = (unsigned)s + 1u;
      unsigned v[32];
      {
        unsigned spins = 0;
        for (;;) {
          bool ok = true;
#pragma unroll
          for (int k = 0; k < 32; ++k) {
            const unsigned long long x =
                __hip_atomic_load(gO + (k >> 2) * 2048 + w * 256 + (k & 3) * 64 + lane, RLX_AGENT);
            v[k] = (unsigned)x;
            ok &= (x >> 32) == epoch;
          }
          if (__all(ok)) break;
          if (spin_fail(spins, tmo, lane)) return;
        }
      }
#pragma unroll
      for (int k = 0; k < 32; ++k)
        *reinterpret_cast<unsigned*>(sO + (k >> 2) * 520 + ((k & 3) * 64 + lane) * 2) = v[k];
      __builtin_amdgcn_s_waitcnt(0xc07f);
      f32x4 acc = (f32x4){0.f, 0.f, 0.f, 0.f};
#pragma unroll
      for (int i = 0; i < 16; ++i) {
        bf16x8 af = *reinterpret_cast<const bf16x8*>(sO + (lane & 7) * 520 + i * 32 + (lane >> 4) * 8);
        acc = __builtin_amdgcn_mfma_f32_16x16x32_bf16(af, wf[i], acc, 0, 0, 0);
      }
      if (lane < 32) {
#pragma unroll
        for (int j = 0; j < 4; ++j) sRed[(w * 8 + (lane >> 4) * 4 + j) * 16 + (lane & 15)] = acc[j];
      }
      __syncthreads();
      if (tid < 64) {
        int r = tid >> 3, cp = tid & 7;
        float v0 = 0.f, v1 = 0.f;
#pragma unroll
        for (int q = 0; q < 8; ++q) {
          v0 += sRed[(q * 8 + r) * 16 + cp * 2];
          v1 += sRed[(q * 8 + r) * 16 + cp * 2 + 1];
        }
        unsigned pk = (unsigned)f2bf(v0) | ((unsigned)f2bf(v1) << 16);
        if (s + 1 < B_NSEG) store_granule(gMem + r * 512 + ob * 8 + cp, epoch, pk);
      }
      __syncthreads();
    }
  }
}

extern "C" void kernel_launch(void* const* d_in, const int* in_sizes, int n_in, void* d_out, int out_size, void* d_ws,
                              size_t ws_size, hipStream_t stream) {
  const u16* A_x = (const u16*)d_in[0];
  const u16* A_wqkv = (const u16*)d_in[1];
  const u16* A_wo = (const u16*)d_in[2];
  const u16* B_x = (const u16*)d_in[3];
  const u16* B_mem0 = (const u16*)d_in[4];
  const u16* B_Wq = (const u16*)d_in[5];
  const u16* B_Wk = (const u16*)d_in[6];
  const u16* B_Wv = (const u16*)d_in[7];
  const u16* B_Wo = (const u16*)d_in[8];
  u16* out = (u16*)d_out;
  char* ws = (char*)d_ws;
  u16* qkvB = (u16*)ws;
  u16* qkvA = (u16*)(ws + (size_t)50331648);
  u16* oA = (u16*)(ws + (size_t)50331648 + 12582912);
  u16* kvm = (u16*)(ws + (size_t)67108864);
  u16* oB = (u16*)(ws + (size_t)75497472);
  char* gran = ws + (size_t)92274688;
  const size_t gran_bytes = 32768 + 262144 + 131072 + 256;
  (void)hipMemsetAsync(gran, 0, gran_bytes, stream);

  gemm128<<<(2048 / 128) * (4096 / 128), 256, 0, stream>>>(B_x, 1024, B_Wq, 4096, qkvB, QKVB_LD, 2048, 4096, 1024);
  gemm128<<<(2048 / 128) * (4096 / 128), 256, 0, stream>>>(B_x, 1024, B_Wk, 4096, qkvB + 4096, QKVB_LD, 2048, 4096, 1024);
  gemm128<<<(2048 / 128) * (4096 / 128), 256, 0, stream>>>(B_x, 1024, B_Wv, 4096, qkvB + 8192, QKVB_LD, 2048, 4096, 1024);
  ChainArgs ca;
  ca.mem0 = B_mem0;
  ca.Wq = B_Wq;
  ca.Wk = B_Wk;
  ca.Wv = B_Wv;
  ca.Wo = B_Wo;
  ca.qkvB = qkvB;
  ca.kvm = kvm;
  ca.gMem = (unsigned long long*)gran;
  ca.gQK = (unsigned long long*)(gran + 32768);
  ca.gO = (unsigned long long*)(gran + 32768 + 262144);
  ca.tmo = (unsigned*)(gran + 32768 + 262144 + 131072);
  (void)hipFuncSetAttribute((const void*)chain_kernel, hipFuncAttributeMaxDynamicSharedMemorySize, 96 * 1024);
  chain_kernel<<<256, 512, 96 * 1024, stream>>>(ca);
  attnBx<<<B_NSEG * 8, 256, 0, stream>>>(qkvB, kvm, oB);
  gemm128<<<(2048 / 128) * (1024 / 128), 256, 0, stream>>>(oB, 4096, B_Wo, 1024, out + 1024, OUT_LD, 2048, 1024, 4096);
  gemm128<<<(2048 / 128) * (3072 / 128), 256, 0, stream>>>(A_x, 1024, A_wqkv, 3072, qkvA, QKVA_LD, 2048, 3072, 1024);
  attnA<<<(2048 / 64) * A_H, 256, 0, stream>>>(qkvA, oA);
  gemm128<<<(2048 / 128) * (1024 / 128), 256, 0, stream>>>(oA, 1024, A_wo, 1024, out, OUT_LD, 2048, 1024, 1024);
}
```

```cpp
#include <hip/hip_runtime.h>
#include <hip/hip_bf16.h>
#include <stdint.h>

constexpr int S_LEN = 2048;
constexpr int D_MODEL = 1024;
constexpr int A_H = 32, A_DH = 32, A_W = 256;
constexpr int B_H = 32, B_DH = 128, B_M = 8, B_SEG = 32, B_NSEG = 64;
constexpr int B_HD = B_H * B_DH;
constexpr int QKVB_LD = 3 * B_HD;
constexpr int QKVA_LD = 3 * D_MODEL;
constexpr int OUT_LD = 2 * D_MODEL;

typedef unsigned short u16;
typedef __attribute__((ext_vector_type(8))) short bf16x8;
typedef __attribute__((ext_vector_type(4))) short bf16x4;
typedef __attribute__((ext_vector_type(4))) float f32x4;
typedef __attribute__((ext_vector_type(4))) unsigned u32x4;
#define GAS __attribute__((address_space(1)))
typedef GAS unsigned gu32;
typedef GAS unsigned long long gu64;
#define RLX_AGENT __ATOMIC_RELAXED, __HIP_MEMORY_SCOPE_AGENT

__device__ __forceinline__ float bf2f(u16 v) { return __uint_as_float(((unsigned)v) << 16); }
__device__ __forceinline__ u16 f2bf(float f) {
  __hip_bfloat16 b = __float2bfloat16(f);
  return *reinterpret_cast<u16*>(&b);
}
__device__ __forceinline__ float rbf(float f) { return bf2f(f2bf(f)); }

__device__ __forceinline__ unsigned boff(unsigned row, unsigned ch) {
  return 256u * row + 16u * (ch ^ (((row & 3) << 2) | ((row >> 2) & 3)));
}

__global__ __launch_bounds__(256, 2) void gemm128(const u16* __restrict__ A, int lda,
                                                  const u16* __restrict__ B, int ldb,
                                                  u16* __restrict__ C, int ldc, int M, int N, int K) {
  __shared__ __attribute__((aligned(16))) char smem[2 * (8192 + 8192)];
  const int nbn = N / 128;
  const int bm = blockIdx.x / nbn, bn = blockIdx.x % nbn;
  const int tid = threadIdx.x, lane = tid & 63, wid = tid >> 6;
  const int wr = wid >> 1, wc = wid & 1;
  const int row0 = bm * 128, col0 = bn * 128;
  u32x4 ra[2], rb[2];
  auto gload = [&](int kt) {
#pragma unroll
    for (int i = 0; i < 2; ++i) {
      int c = tid + 256 * i;
      int r = c >> 2, ch = c & 3;
      ra[i] = *reinterpret_cast<const u32x4*>(A + (size_t)(row0 + r) * lda + kt * 32 + ch * 8);
      int kr = c >> 4, chb = c & 15;
      rb[i] = *reinterpret_cast<const u32x4*>(B + (size_t)(kt * 32 + kr) * ldb + col0 + chb * 8);
    }
  };
  auto swrite = [&](int buf) {
    char* sa = smem + buf * 16384;
    char* sb = sa + 8192;
#pragma unroll
    for (int i = 0; i < 2; ++i) {
      int c = tid + 256 * i;
      int r = c >> 2, ch = c & 3;
      *reinterpret_cast<u32x4*>(sa + r * 64 + ch * 16) = ra[i];
      int kr = c >> 4, chb = c & 15;
      *reinterpret_cast<u32x4*>(sb + boff(kr, chb)) = rb[i];
    }
  };
  f32x4 acc[4][4];
#pragma unroll
  for (int m = 0; m < 4; ++m)
#pragma unroll
    for (int n = 0; n < 4; ++n) acc[m][n] = (f32x4){0.f, 0.f, 0.f, 0.f};
  const int nk = K / 32;
  gload(0);
  swrite(0);
  __syncthreads();
  for (int kt = 0; kt < nk; ++kt) {
    const int buf = kt & 1;
    if (kt + 1 < nk) gload(kt + 1);
    const char* sa = smem + buf * 16384;
    const char* sb = sa + 8192;
    bf16x8 af[4], bfr[4];
#pragma unroll
    for (int m = 0; m < 4; ++m)
      af[m] = *reinterpret_cast<const bf16x8*>(sa + (wr * 64 + m * 16 + (lane & 15)) * 64 + (lane >> 4) * 16);
    {
      const unsigned g = lane >> 4, q = (lane & 15) >> 2, p = lane & 3;
      const unsigned base = (unsigned)(size_t)sb;
#pragma unroll
      for (int n = 0; n < 4; ++n) {
        const unsigned c = wc * 4 + n;
        unsigned a0 = base + boff(8 * g + 0 + q, 2 * c + (p >> 1)) + 8 * (p & 1);
        unsigned a1 = base + boff(8 * g + 4 + q, 2 * c + (p >> 1)) + 8 * (p & 1);
        bf16x4 lo, hi;
        asm volatile("ds_read_b64_tr_b16 %0, %2\n\tds_read_b64_tr_b16 %1, %3\n\ts_waitcnt lgkmcnt(0)"
                     : "=&v"(lo), "=&v"(hi)
                     : "v"(a0), "v"(a1)
                     : "memory");
        bfr[n] = (bf16x8){lo[0], lo[1], lo[2], lo[3], hi[0], hi[1], hi[2], hi[3]};
      }
    }
#pragma unroll
    for (int m = 0; m < 4; ++m)
#pragma unroll
      for (int n = 0; n < 4; ++n)
        acc[m][n] = __builtin_amdgcn_mfma_f32_16x16x32_bf16(af[m], bfr[n], acc[m][n], 0, 0, 0);
    if (kt + 1 < nk) swrite(buf ^ 1);
    __syncthreads();
  }
#pragma unroll
  for (int m = 0; m < 4; ++m)
#pragma unroll
    for (int n = 0; n < 4; ++n)
#pragma unroll
      for (int j = 0; j < 4; ++j)
        C[(size_t)(row0 + wr * 64 + m * 16 + (lane >> 4) * 4 + j) * ldc + col0 + wc * 64 + n * 16 + (lane & 15)] =
            f2bf(acc[m][n][j]);
}

__global__ __launch_bounds__(256, 2) void attnA(const u16* __restrict__ qkv, u16* __restrict__ o) {
  const int h = blockIdx.x % A_H, qb = blockIdx.x / A_H;
  const int tid = threadIdx.x, lane = tid & 63, wid = tid >> 6;
  const int q0 = qb * 64;
  const int kbase = q0 - 256;
  __shared__ __attribute__((aligned(16))) u16 sK[320 * 32];
  __shared__ __attribute__((aligned(16))) u16 sV[320 * 32];
  __shared__ __attribute__((aligned(16))) u16 sP[4][16 * 296];
  for (int c = tid; c < 320 * 4; c += 256) {
    int r = c >> 2, ch = c & 3;
    int key = kbase + r;
    u32x4 kv = (u32x4){0, 0, 0, 0}, vv = (u32x4){0, 0, 0, 0};
    if (key >= 0) {
      kv = *reinterpret_cast<const u32x4*>(qkv + (size_t)key * QKVA_LD + D_MODEL + h * 32 + ch * 8);
      vv = *reinterpret_cast<const u32x4*>(qkv + (size_t)key * QKVA_LD + 2 * D_MODEL + h * 32 + ch * 8);
    }
    *reinterpret_cast<u32x4*>(sK + r * 32 + ch * 8) = kv;
    *reinterpret_cast<u32x4*>(sV + r * 32 + ch * 8) = vv;
  }
  __syncthreads();
  const int qr0 = q0 + wid * 16;
  bf16x8 qf = *reinterpret_cast<const bf16x8*>(qkv + (size_t)(qr0 + (lane & 15)) * QKVA_LD + h * 32 + (lane >> 4) * 8);
  const int t0 = wid;
  f32x4 sc[17];
  const float scale = 0.17677669529663687f;
  float mx[4] = {-3e38f, -3e38f, -3e38f, -3e38f};
#pragma unroll
  for (int t = 0; t < 17; ++t) {
    bf16x8 kf = *reinterpret_cast<const bf16x8*>(sK + ((t0 + t) * 16 + (lane & 15)) * 32 + (lane >> 4) * 8);
    f32x4 s = __builtin_amdgcn_mfma_f32_16x16x32_bf16(qf, kf, (f32x4){0.f, 0.f, 0.f, 0.f}, 0, 0, 0);
    const int key = kbase + (t0 + t) * 16 + (lane & 15);
#pragma unroll
    for (int j = 0; j < 4; ++j) {
      const int row = qr0 + (lane >> 4) * 4 + j;
      float v = rbf(rbf(s[j]) * scale);
      const bool ok = (key <= row) && (row - key < A_W) && (key >= 0);
      v = ok ? v : -1e9f;
      s[j] = v;
      mx[j] = fmaxf(mx[j], v);
    }
    sc[t] = s;
  }
#pragma unroll
  for (int j = 0; j < 4; ++j) {
#pragma unroll
    for (int m = 1; m < 16; m <<= 1) mx[j] = fmaxf(mx[j], __shfl_xor(mx[j], m));
  }
  float sum[4] = {0.f, 0.f, 0.f, 0.f};
#pragma unroll
  for (int t = 0; t < 17; ++t)
#pragma unroll
    for (int j = 0; j < 4; ++j) {
      float e = __expf(sc[t][j] - mx[j]);
      sc[t][j] = e;
      sum[j] += e;
    }
#pragma unroll
  for (int j = 0; j < 4; ++j) {
#pragma unroll
    for (int m = 1; m < 16; m <<= 1) sum[j] += __shfl_xor(sum[j], m);
    sum[j] = 1.f / sum[j];
  }
  u16* P = sP[wid];
#pragma unroll
  for (int t = 0; t < 17; ++t)
#pragma unroll
    for (int j = 0; j < 4; ++j) P[((lane >> 4) * 4 + j) * 296 + t * 16 + (lane & 15)] = f2bf(sc[t][j] * sum[j]);
#pragma unroll
  for (int j = 0; j < 4; ++j) P[((lane >> 4) * 4 + j) * 296 + 272 + (lane & 15)] = 0;
  __syncthreads();
  f32x4 oacc[2] = {(f32x4){0.f, 0.f, 0.f, 0.f}, (f32x4){0.f, 0.f, 0.f, 0.f}};
  for (int ks = 0; ks < 9; ++ks) {
    bf16x8 pf = *reinterpret_cast<const bf16x8*>(P + (lane & 15) * 296 + ks * 32 + (lane >> 4) * 8);
#pragma unroll
    for (int n = 0; n < 2; ++n) {
      bf16x8 vf;
#pragma unroll
      for (int j = 0; j < 8; ++j) {
        int kl = t0 * 16 + ks * 32 + (lane >> 4) * 8 + j;
        kl = kl < 320 ? kl : 319;
        vf[j] = (short)sV[kl * 32 + n * 16 + (lane & 15)];
      }
      oacc[n] = __builtin_amdgcn_mfma_f32_16x16x32_bf16(pf, vf, oacc[n], 0, 0, 0);
    }
  }
#pragma unroll
  for (int n = 0; n < 2; ++n)
#pragma unroll
    for (int j = 0; j < 4; ++j)
      o[(size_t)(qr0 + (lane >> 4) * 4 + j) * D_MODEL + h * 32 + n * 16 + (lane & 15)] = f2bf(oacc[n][j]);
}

__global__ __launch_bounds__(256, 2) void attnBx(const u16* __restrict__ qkv, const u16* __restrict__ kvm,
                                                 u16* __restrict__ o) {
  const int seg = blockIdx.x / 8, hg = blockIdx.x % 8;
  const int tid = threadIdx.x, lane = tid & 63, wid = tid >> 6;
  const int h = hg * 4 + wid;
  __shared__ __attribute__((aligned(16))) u16 sK[4][48 * 136];
  __shared__ __attribute__((aligned(16))) u16 sV[4][64 * 136];
  __shared__ __attribute__((aligned(16))) u16 sP[4][32 * 72];
  u16* K = sK[wid];
  u16* V = sV[wid];
  u16* P = sP[wid];
  for (int c = lane; c < 64 * 16; c += 64) {
    int r = c >> 4, ch = c & 15;
    u32x4 kv = (u32x4){0, 0, 0, 0}, vv = (u32x4){0, 0, 0, 0};
    if (r < 8) {
      kv = *reinterpret_cast<const u32x4*>(kvm + ((size_t)(seg * 2 + 0) * 8 + r) * B_HD + h * 128 + ch * 8);
      vv = *reinterpret_cast<const u32x4*>(kvm + ((size_t)(seg * 2 + 1) * 8 + r) * B_HD + h * 128 + ch * 8);
    } else if (r < 40) {
      const u16* rowp = qkv + (size_t)(seg * 32 + r - 8) * QKVB_LD;
      kv = *reinterpret_cast<const u32x4*>(rowp + B_HD + h * 128 + ch * 8);
      vv = *reinterpret_cast<const u32x4*>(rowp + 2 * B_HD + h * 128 + ch * 8);
    }
    if (r < 48) *reinterpret_cast<u32x4*>(K + r * 136 + ch * 8) = kv;
    *reinterpret_cast<u32x4*>(V + r * 136 + ch * 8) = vv;
  }
  __syncthreads();
  const float scale = 0.08838834764831845f;
#pragma unroll
  for (int mt = 0; mt < 2; ++mt) {
    bf16x8 qf[4];
#pragma unroll
    for (int ks = 0; ks < 4; ++ks)
      qf[ks] = *reinterpret_cast<const bf16x8*>(qkv + (size_t)(seg * 32 + mt * 16 + (lane & 15)) * QKVB_LD + h * 128 +
                                                ks * 32 + (lane >> 4) * 8);
    f32x4 sc[3];
    float mx[4] = {-3e38f, -3e38f, -3e38f, -3e38f};
#pragma unroll
    for (int t = 0; t < 3; ++t) {
      f32x4 s = (f32x4){0.f, 0.f, 0.f, 0.f};
#pragma unroll
      for (int ks = 0; ks < 4; ++ks) {
        bf16x8 kf = *reinterpret_cast<const bf16x8*>(K + (t * 16 + (lane & 15)) * 136 + ks * 32 + (lane >> 4) * 8);
        s = __builtin_amdgcn_mfma_f32_16x16x32_bf16(qf[ks], kf, s, 0, 0, 0);
      }
      const int key = t * 16 + (lane & 15);
#pragma unroll
      for (int j = 0; j < 4; ++j) {
        const int rowp = 8 + mt * 16 + (lane >> 4) * 4 + j;
        float v = rbf(rbf(s[j]) * scale);
        const bool ok = (key <= rowp);
        v = ok ? v : -1e9f;
        s[j] = v;
        mx[j] = fmaxf(mx[j], v);
      }
      sc[t] = s;
    }
#pragma unroll
    for (int j = 0; j < 4; ++j) {
#pragma unroll
      for (int m = 1; m < 16; m <<= 1) mx[j] = fmaxf(mx[j], __shfl_xor(mx[j], m));
    }
    float sum[4] = {0.f, 0.f, 0.f, 0.f};
#pragma unroll
    for (int t = 0; t < 3; ++t)
#pragma unroll
      for (int j = 0; j < 4; ++j) {
        float e = __expf(sc[t][j] - mx[j]);
        sc[t][j] = e;
        sum[j] += e;
      }
#pragma unroll
    for (int j = 0; j < 4; ++j) {
#pragma unroll
      for (int m = 1; m < 16; m <<= 1) sum[j] += __shfl_xor(sum[j], m);
      sum[j] = 1.f / sum[j];
    }
#pragma unroll
    for (int t = 0; t < 3; ++t)
#pragma unroll
      for (int j = 0; j < 4; ++j)
        P[(mt * 16 + (lane >> 4) * 4 + j) * 72 + t * 16 + (lane & 15)] = f2bf(sc[t][j] * sum[j]);
#pragma unroll
    for (int j = 0; j < 4; ++j) P[(mt * 16 + (lane >> 4) * 4 + j) * 72 + 48 + (lane & 15)] = 0;
  }
  __syncthreads();
#pragma unroll
  for (int mt = 0; mt < 2; ++mt) {
    bf16x8 pf[2];
#pragma unroll
    for (int ks = 0; ks < 2; ++ks)
      pf[ks] = *reinterpret_cast<const bf16x8*>(P + (mt * 16 + (lane & 15)) * 72 + ks * 32 + (lane >> 4) * 8);
#pragma unroll
    for (int n = 0; n < 8; ++n) {
      f32x4 oacc = (f32x4){0.f, 0.f, 0.f, 0.f};
#pragma unroll
      for (int ks = 0; ks < 2; ++ks) {
        bf16x8 vf;
#pragma unroll
        for (int j = 0; j < 8; ++j) vf[j] = (short)V[(ks * 32 + (lane >> 4) * 8 + j) * 136 + n * 16 + (lane & 15)];
        oacc = __builtin_amdgcn_mfma_f32_16x16x32_bf16(pf[ks], vf, oacc, 0, 0, 0);
      }
#pragma unroll
      for (int j = 0; j < 4; ++j)
        o[(size_t)(seg * 32 + mt * 16 + (lane >> 4) * 4 + j) * B_HD + h * 128 + n * 16 + (lane & 15)] = f2bf(oacc[j]);
    }
  }
}

constexpr unsigned kSpinCap = 1u << 22;

__device__ __forceinline__ bool spin_fail(unsigned& spins, gu32* tmo, int lane) {
  __builtin_amdgcn_s_sleep(1);
  if (++spins > kSpinCap) {
    if (lane == 0) __hip_atomic_store(tmo, 1u, RLX_AGENT);
    return true;
  }
  return (spins & 255u) == 0u && __builtin_amdgcn_readfirstlane(__hip_atomic_load(tmo, RLX_AGENT));
}
__device__ __forceinline__ void store_granule(gu64* g, unsigned epoch, unsigned value) {
  __hip_atomic_store(g, ((unsigned long long)epoch << 32) | value, RLX_AGENT);
}
template <int N>
__device__ __forceinline__ bool sweep_granules(gu64* g, int S, unsigned epoch, unsigned (&v)[N], gu32* tmo, int lane) {
  for (unsigned spins = 0;;) {
    bool ok = true;
#pragma unroll
    for (int k = 0; k < N; ++k) {
      const unsigned long long x = __hip_atomic_load(g + k * S, RLX_AGENT);
      v[k] = (unsigned)x;
      ok &= (x >> 32) == epoch;
    }
    if (__all(ok)) return true;
    if (spin_fail(spins, tmo, lane)) return false;
  }
}

struct ChainArgs {
  const u16* mem0;
  const u16* Wq;
  const u16* Wk;
  const u16* Wv;
  const u16* Wo;
  const u16* qkvB;
  u16* kvm;
  unsigned long long* gMem;
  unsigned long long* gQK;
  float* macc;
  unsigned* tmo;
};

#define STAMP(idx) do {} while (0)
constexpr int MEM_LDS_STRIDE = 1032;

__global__ __launch_bounds__(256, 1) void chain_kernel(ChainArgs a) {
  extern __shared__ __attribute__((aligned(16))) char lds[];
  const int b = blockIdx.x;
  const int tid = threadIdx.x, lane = tid & 63;
  const int w = __builtin_amdgcn_readfirstlane(tid >> 6);
  gu64* gMem = (gu64*)a.gMem;
  gu64* gQK = (gu64*)a.gQK;
  gu32* tmo = (gu32*)a.tmo;

  if (b < 192) {
    const int t = b / 64, cb = b % 64;
    const u16* W = t == 0 ? a.Wq : (t == 1 ? a.Wk : a.Wv);
    const int col = cb * 64 + w * 16 + (lane & 15);
    bf16x8 wf[32];
#pragma unroll
    for (int i = 0; i < 32; ++i) {
#pragma unroll
      for (int j = 0; j < 8; ++j) wf[i][j] = (short)W[(size_t)(i * 32 + (lane >> 4) * 8 + j) * B_HD + col];
      asm volatile("" : "+v"(wf[i]));
    }
    bf16x8 wo[16][2];
#pragma unroll
    for (int i = 0; i < 16; ++i)
#pragma unroll
      for (int ks = 0; ks < 2; ++ks) {
#pragma unroll
        for (int j = 0; j < 8; ++j)
          wo[i][ks][j] = t == 2 ? (short)a.Wo[(size_t)(cb * 64 + ks * 32 + (lane >> 4) * 8 + j) * D_MODEL + w * 256 + i * 16 + (lane & 15)] : (short)0;
        asm volatile("" : "+v"(wo[i][ks]));
      }
    u16* sMem = (u16*)lds;
    u16* sQ = (u16*)(lds + 16896);
    u16* sK = sQ + 8 * 136;
    u16* sOo = sK + 48 * 136 + 64 * 56;
    u16* sVT = sK + 48 * 136;
    if (t == 2) {
      for (int i = tid; i < 8 * 136; i += 256) sK[40 * 136 + i] = 0;
      for (int i = tid; i < 64 * 8; i += 256) sVT[(i >> 3) * 56 + 40 + (i & 7)] = 0;
    }
    const int h = cb >> 1, half = cb & 1;
    for (int s = 0; s < B_NSEG; ++s) {
      const unsigned epoch = (unsigned)s + 1u;
      u32x4 kxr0 = (u32x4){0, 0, 0, 0}, kxr1 = (u32x4){0, 0, 0, 0}, vxr = (u32x4){0, 0, 0, 0};
      if (t == 2) {
        {
          int r = tid >> 4, ch = tid & 15;
          kxr0 = *reinterpret_cast<const u32x4*>(a.qkvB + (size_t)(s * 32 + r) * QKVB_LD + B_HD + h * 128 + ch * 8);
          kxr1 = *reinterpret_cast<const u32x4*>(a.qkvB + (size_t)(s * 32 + 16 + r) * QKVB_LD + B_HD + h * 128 + ch * 8);
        }
        {
          int r = tid >> 3, ch = tid & 7;
          vxr = *reinterpret_cast<const u32x4*>(a.qkvB + (size_t)(s * 32 + r) * QKVB_LD + 2 * B_HD + h * 128 +
                                                half * 64 + ch * 8);
        }
      }
      STAMP(0);
      if (s == 0) {
#pragma unroll
        for (int k = 0; k < 16; ++k) {
          int g = tid + 256 * k;
          int r = g >> 9, cp = g & 511;
          unsigned v = *reinterpret_cast<const unsigned*>(a.mem0 + r * 1024 + cp * 2);
          *reinterpret_cast<unsigned*>(sMem + r * MEM_LDS_STRIDE + cp * 2) = v;
        }
      } else {
        {
          unsigned spins = 0;
          for (;;) {
            const unsigned c = __hip_atomic_load((gu32*)a.gMem + (size_t)(s - 1) * 64 + (b & 7) * 8, RLX_AGENT);
            if (__builtin_amdgcn_readfirstlane(c) == 64u) break;
            if (spin_fail(spins, tmo, lane)) return;
          }
        }
        const gu64* src = (const gu64*)(a.macc + (size_t)(s - 1) * 8192);
        unsigned long long v[16];
#pragma unroll
        for (int k = 0; k < 16; ++k) v[k] = __hip_atomic_load(src + tid + 256 * k, RLX_AGENT);
#pragma unroll
        for (int k = 0; k < 16; ++k) {
          int g = tid + 256 * k;
          int r = g >> 9, cp = g & 511;
          *reinterpret_cast<unsigned*>(sMem + r * MEM_LDS_STRIDE + cp * 2) =
              (unsigned)f2bf(__uint_as_float((unsigned)v[k])) | ((unsigned)f2bf(__uint_as_float((unsigned)(v[k] >> 32))) << 16);
        }
      }
      __syncthreads();
      STAMP(1);
      f32x4 acc = (f32x4){0.f, 0.f, 0.f, 0.f}, acc2 = (f32x4){0.f, 0.f, 0.f, 0.f};
      {
        bf16x8 af[32];
#pragma unroll
        for (int i = 0; i < 32; ++i)
          af[i] = *reinterpret_cast<const bf16x8*>(sMem + (lane & 7) * MEM_LDS_STRIDE + i * 32 + (lane >> 4) * 8);
#pragma unroll
        for (int i = 0; i < 32; i += 2) {
          acc = __builtin_amdgcn_mfma_f32_16x16x32_bf16(wf[i], af[i], acc, 0, 0, 0);
          acc2 = __builtin_amdgcn_mfma_f32_16x16x32_bf16(wf[i + 1], af[i + 1], acc2, 0, 0, 0);
        }
        acc += acc2;
      }
      {
        if ((lane & 15) < 8) {
          const int r = lane & 15;
#pragma unroll
          for (int jj = 0; jj < 2; ++jj) {
            const unsigned pk = (unsigned)f2bf(acc[2 * jj]) | ((unsigned)f2bf(acc[2 * jj + 1]) << 16);
            const int cp = w * 8 + (lane >> 4) * 2 + jj;
            if (t < 2) store_granule(gQK + (size_t)t * 16384 + r * 2048 + cb * 32 + cp, epoch, pk);
            if (t >= 1)
              *reinterpret_cast<unsigned*>(a.kvm + ((size_t)(s * 2 + (t - 1)) * 8 + r) * B_HD + cb * 64 + cp * 2) = pk;
            if (t == 2) {
              sVT[(cp * 2) * 56 + r] = (u16)(pk & 0xffff);
              sVT[(cp * 2 + 1) * 56 + r] = (u16)(pk >> 16);
            }
          }
        }
      }
      STAMP(2);
      if (t == 2) {
        {
          int r = tid >> 4, ch = tid & 15;
          *reinterpret_cast<u32x4*>(sK + (8 + r) * 136 + ch * 8) = kxr0;
          *reinterpret_cast<u32x4*>(sK + (24 + r) * 136 + ch * 8) = kxr1;
        }
        {
          int r = tid >> 3, ch = tid & 7;
#pragma unroll
          for (int e = 0; e < 4; ++e) {
            sVT[(ch * 8 + 2 * e) * 56 + 8 + r] = (u16)(vxr[e] & 0xffff);
            sVT[(ch * 8 + 2 * e + 1) * 56 + 8 + r] = (u16)(vxr[e] >> 16);
          }
        }
        {
          unsigned v[4];
          int r = tid >> 5, cp = tid & 31;
          {
            unsigned spins = 0;
            for (;;) {
              bool ok = true;
#pragma unroll
              for (int k = 0; k < 4; ++k) {
                const unsigned long long x = __hip_atomic_load(
                    gQK + (size_t)(k >> 1) * 16384 + r * 2048 + h * 64 + (k & 1) * 32 + cp, RLX_AGENT);
                v[k] = (unsigned)x;
                ok &= (x >> 32) == epoch;
              }
              if (__all(ok)) break;
              if (spin_fail(spins, tmo, lane)) return;
            }
          }
          *reinterpret_cast<unsigned*>(sQ + r * 136 + cp * 2) = v[0];
          *reinterpret_cast<unsigned*>(sQ + r * 136 + 64 + cp * 2) = v[1];
          *reinterpret_cast<unsigned*>(sK + r * 136 + cp * 2) = v[2];
          *reinterpret_cast<unsigned*>(sK + r * 136 + 64 + cp * 2) = v[3];
        }
        __syncthreads();
        STAMP(3);
        if (w == 0) {
          bf16x8 qf[4], kf[3][4];
#pragma unroll
          for (int ks = 0; ks < 4; ++ks)
            qf[ks] = *reinterpret_cast<const bf16x8*>(sQ + (lane & 7) * 136 + ks * 32 + (lane >> 4) * 8);
#pragma unroll
          for (int tt = 0; tt < 3; ++tt)
#pragma unroll
            for (int ks = 0; ks < 4; ++ks)
              kf[tt][ks] = *reinterpret_cast<const bf16x8*>(sK + (tt * 16 + (lane & 15)) * 136 + ks * 32 + (lane >> 4) * 8);
          bf16x4 vlo[4], vmid[4], vhi[4];
#pragma unroll
          for (int n = 0; n < 4; ++n) {
            const u16* vp = sVT + (n * 16 + (lane & 15)) * 56 + (lane >> 4) * 4;
            vlo[n] = *reinterpret_cast<const bf16x4*>(vp);
            vmid[n] = *reinterpret_cast<const bf16x4*>(vp + 16);
            vhi[n] = *reinterpret_cast<const bf16x4*>(vp + 32);
          }
          f32x4 sc[3];
          float mx = -3e38f;
#pragma unroll
          for (int tt = 0; tt < 3; ++tt) {
            f32x4 sv = (f32x4){0.f, 0.f, 0.f, 0.f};
#pragma unroll
            for (int ks = 0; ks < 4; ++ks) sv = __builtin_amdgcn_mfma_f32_16x16x32_bf16(kf[tt][ks], qf[ks], sv, 0, 0, 0);
#pragma unroll
            for (int j = 0; j < 4; ++j) {
              const int key = tt * 16 + (lane >> 4) * 4 + j;
              float v = rbf(rbf(sv[j]) * 0.08838834764831845f);
              v = key < 40 ? v : -3e38f;
              sv[j] = v;
              mx = fmaxf(mx, v);
            }
            sc[tt] = sv;
          }
          mx = fmaxf(mx, __shfl_xor(mx, 16));
          mx = fmaxf(mx, __shfl_xor(mx, 32));
          float sum = 0.f;
#pragma unroll
          for (int tt = 0; tt < 3; ++tt)
#pragma unroll
            for (int j = 0; j < 4; ++j) {
              float e = __expf(sc[tt][j] - mx);
              sc[tt][j] = e;
              sum += e;
            }
          sum += __shfl_xor(sum, 16);
          sum += __shfl_xor(sum, 32);
          const float inv = 1.f / sum;
          bf16x8 p0, p1;
#pragma unroll
          for (int j = 0; j < 4; ++j) {
            p0[j] = (short)f2bf(sc[0][j] * inv);
            p0[4 + j] = (short)f2bf(sc[1][j] * inv);
            p1[j] = (short)f2bf(sc[2][j] * inv);
            p1[4 + j] = 0;
          }
#pragma unroll
          for (int n = 0; n < 4; ++n) {
            bf16x8 va = (bf16x8){vlo[n][0], vlo[n][1], vlo[n][2], vlo[n][3], vmid[n][0], vmid[n][1], vmid[n][2], vmid[n][3]};
            bf16x8 vb = (bf16x8){vhi[n][0], vhi[n][1], vhi[n][2], vhi[n][3], 0, 0, 0, 0};
            f32x4 oacc = __builtin_amdgcn_mfma_f32_16x16x32_bf16(va, p0, (f32x4){0.f, 0.f, 0.f, 0.f}, 0, 0, 0);
            oacc = __builtin_amdgcn_mfma_f32_16x16x32_bf16(vb, p1, oacc, 0, 0, 0);
            if ((lane & 15) < 8) {
#pragma unroll
              for (int jj = 0; jj < 2; ++jj)
                *reinterpret_cast<unsigned*>(sOo + (lane & 15) * 72 + n * 16 + (lane >> 4) * 4 + jj * 2) =
                    (unsigned)f2bf(oacc[2 * jj]) | ((unsigned)f2bf(oacc[2 * jj + 1]) << 16);
            }
          }
        }
        __syncthreads();
        if (s + 1 < B_NSEG) {
          bf16x8 of0 = *reinterpret_cast<const bf16x8*>(sOo + (lane & 7) * 72 + (lane >> 4) * 8);
          bf16x8 of1 = *reinterpret_cast<const bf16x8*>(sOo + (lane & 7) * 72 + 32 + (lane >> 4) * 8);
          float* dst = a.macc + (size_t)s * 8192 + (lane & 15) * 1024 + w * 256 + (lane >> 4) * 4;
#pragma unroll
          for (int i = 0; i < 16; ++i) {
            f32x4 pa = __builtin_amdgcn_mfma_f32_16x16x32_bf16(wo[i][0], of0, (f32x4){0.f, 0.f, 0.f, 0.f}, 0, 0, 0);
            pa = __builtin_amdgcn_mfma_f32_16x16x32_bf16(wo[i][1], of1, pa, 0, 0, 0);
            if ((lane & 15) < 8) {
#pragma unroll
              for (int j = 0; j < 4; ++j)
                __hip_atomic_fetch_add((GAS float*)(dst + i * 16 + j), pa[j], RLX_AGENT);
            }
          }
          asm volatile("s_waitcnt vmcnt(0)" ::: "memory");
          __syncthreads();
          if (tid < 8) __hip_atomic_fetch_add((gu32*)a.gMem + (size_t)s * 64 + tid * 8, 1u, RLX_AGENT);
        }
        STAMP(4);
      }
      __syncthreads();
    }
  }
}

extern "C" void kernel_launch(void* const* d_in, const int* in_sizes, int n_in, void* d_out, int out_size, void* d_ws,
                              size_t ws_size, hipStream_t stream) {
  const u16* A_x = (const u16*)d_in[0];
  const u16* A_wqkv = (const u16*)d_in[1];
  const u16* A_wo = (const u16*)d_in[2];
  const u16* B_x = (const u16*)d_in[3];
  const u16* B_mem0 = (const u16*)d_in[4];
  const u16* B_Wq = (const u16*)d_in[5];
  const u16* B_Wk = (const u16*)d_in[6];
  const u16* B_Wv = (const u16*)d_in[7];
  const u16* B_Wo = (const u16*)d_in[8];
  u16* out = (u16*)d_out;
  char* ws = (char*)d_ws;
  u16* qkvB = (u16*)ws;
  u16* qkvA = (u16*)(ws + (size_t)50331648);
  u16* oA = (u16*)(ws + (size_t)50331648 + 12582912);
  u16* kvm = (u16*)(ws + (size_t)67108864);
  u16* oB = (u16*)(ws + (size_t)75497472);
  char* gran = ws + (size_t)92274688;
  const size_t gran_bytes = 32768 + 262144 + 131072 + 256 + 2097152;
  (void)hipMemsetAsync(gran, 0, gran_bytes, stream);

  gemm128<<<(2048 / 128) * (4096 / 128), 256, 0, stream>>>(B_x, 1024, B_Wq, 4096, qkvB, QKVB_LD, 2048, 4096, 1024);
  gemm128<<<(2048 / 128) * (4096 / 128), 256, 0, stream>>>(B_x, 1024, B_Wk, 4096, qkvB + 4096, QKVB_LD, 2048, 4096, 1024);
  gemm128<<<(2048 / 128) * (4096 / 128), 256, 0, stream>>>(B_x, 1024, B_Wv, 4096, qkvB + 8192, QKVB_LD, 2048, 4096, 1024);
  ChainArgs ca;
  ca.mem0 = B_mem0;
  ca.Wq = B_Wq;
  ca.Wk = B_Wk;
  ca.Wv = B_Wv;
  ca.Wo = B_Wo;
  ca.qkvB = qkvB;
  ca.kvm = kvm;
  ca.gMem = (unsigned long long*)gran;
  ca.gQK = (unsigned long long*)(gran + 32768);
  ca.macc = (float*)(gran + 32768 + 262144 + 131072 + 256);
  ca.tmo = (unsigned*)(gran + 32768 + 262144 + 131072);
  (void)hipFuncSetAttribute((const void*)chain_kernel, hipFuncAttributeMaxDynamicSharedMemorySize, 96 * 1024);
  chain_kernel<<<192, 256, 96 * 1024, stream>>>(ca);
  attnBx<<<B_NSEG * 8, 256, 0, stream>>>(qkvB, kvm, oB);
  gemm128<<<(2048 / 128) * (1024 / 128), 256, 0, stream>>>(oB, 4096, B_Wo, 1024, out + 1024, OUT_LD, 2048, 1024, 4096);
  gemm128<<<(2048 / 128) * (3072 / 128), 256, 0, stream>>>(A_x, 1024, A_wqkv, 3072, qkvA, QKVA_LD, 2048, 3072, 1024);
  attnA<<<(2048 / 64) * A_H, 256, 0, stream>>>(qkvA, oA);
  gemm128<<<(2048 / 128) * (1024 / 128), 256, 0, stream>>>(oA, 1024, A_wo, 1024, out, OUT_LD, 2048, 1024, 1024);
}
```

```cpp
#include <hip/hip_runtime.h>
#include <hip/hip_bf16.h>
#include <stdint.h>

constexpr int S_LEN = 2048;
constexpr int D_MODEL = 1024;
constexpr int A_H = 32, A_DH = 32, A_W = 256;
constexpr int B_H = 32, B_DH = 128, B_M = 8, B_SEG = 32, B_NSEG = 64;
constexpr int B_HD = B_H * B_DH;
constexpr int QKVB_LD = 3 * B_HD;
constexpr int QKVA_LD = 3 * D_MODEL;
constexpr int OUT_LD = 2 * D_MODEL;

typedef unsigned short u16;
typedef __attribute__((ext_vector_type(8))) short bf16x8;
typedef __attribute__((ext_vector_type(4))) short bf16x4;
typedef __attribute__((ext_vector_type(4))) float f32x4;
typedef __attribute__((ext_vector_type(4))) unsigned u32x4;
#define GAS __attribute__((address_space(1)))
typedef GAS unsigned gu32;
typedef GAS unsigned long long gu64;
#define RLX_AGENT __ATOMIC_RELAXED, __HIP_MEMORY_SCOPE_AGENT

__device__ __forceinline__ float bf2f(u16 v) { return __uint_as_float(((unsigned)v) << 16); }
__device__ __forceinline__ u16 f2bf(float f) {
  __hip_bfloat16 b = __float2bfloat16(f);
  return *reinterpret_cast<u16*>(&b);
}
__device__ __forceinline__ float rbf(float f) { return bf2f(f2bf(f)); }

__device__ __forceinline__ unsigned boff(unsigned row, unsigned ch) {
  return 256u * row + 16u * (ch ^ (((row & 3) << 2) | ((row >> 2) & 3)));
}

__global__ __launch_bounds__(256, 2) void gemm128(const u16* __restrict__ A, int lda,
                                                  const u16* __restrict__ B, int ldb,
                                                  u16* __restrict__ C, int ldc, int M, int N, int K) {
  __shared__ __attribute__((aligned(16))) char smem[2 * (8192 + 8192)];
  const int nbn = N / 128;
  const int bm = blockIdx.x / nbn, bn = blockIdx.x % nbn;
  const int tid = threadIdx.x, lane = tid & 63, wid = tid >> 6;
  const int wr = wid >> 1, wc = wid & 1;
  const int row0 = bm * 128, col0 = bn * 128;
  u32x4 ra[2], rb[2];
  auto gload = [&](int kt) {
#pragma unroll
    for (int i = 0; i < 2; ++i) {
      int c = tid + 256 * i;
      int r = c >> 2, ch = c & 3;
      ra[i] = *reinterpret_cast<const u32x4*>(A + (size_t)(row0 + r) * lda + kt * 32 + ch * 8);
      int kr = c >> 4, chb = c & 15;
      rb[i] = *reinterpret_cast<const u32x4*>(B + (size_t)(kt * 32 + kr) * ldb + col0 + chb * 8);
    }
  };
  auto swrite = [&](int buf) {
    char* sa = smem + buf * 16384;
    char* sb = sa + 8192;
#pragma unroll
    for (int i = 0; i < 2; ++i) {
      int c = tid + 256 * i;
      int r = c >> 2, ch = c & 3;
      *reinterpret_cast<u32x4*>(sa + r * 64 + ch * 16) = ra[i];
      int kr = c >> 4, chb = c & 15;
      *reinterpret_cast<u32x4*>(sb + boff(kr, chb)) = rb[i];
    }
  };
  f32x4 acc[4][4];
#pragma unroll
  for (int m = 0; m < 4; ++m)
#pragma unroll
    for (int n = 0; n < 4; ++n) acc[m][n] = (f32x4){0.f, 0.f, 0.f, 0.f};
  const int nk = K / 32;
  gload(0);
  swrite(0);
  __syncthreads();
  for (int kt = 0; kt < nk; ++kt) {
    const int buf = kt & 1;
    if (kt + 1 < nk) gload(kt + 1);
    const char* sa = smem + buf * 16384;
    const char* sb = sa + 8192;
    bf16x8 af[4], bfr[4];
#pragma unroll
    for (int m = 0; m < 4; ++m)
      af[m] = *reinterpret_cast<const bf16x8*>(sa + (wr * 64 + m * 16 + (lane & 15)) * 64 + (lane >> 4) * 16);
    {
      const unsigned g = lane >> 4, q = (lane & 15) >> 2, p = lane & 3;
      const unsigned base = (unsigned)(size_t)sb;
#pragma unroll
      for (int n = 0; n < 4; ++n) {
        const unsigned c = wc * 4 + n;
        unsigned a0 = base + boff(8 * g + 0 + q, 2 * c + (p >> 1)) + 8 * (p & 1);
        unsigned a1 = base + boff(8 * g + 4 + q, 2 * c + (p >> 1)) + 8 * (p & 1);
        bf16x4 lo, hi;
        asm volatile("ds_read_b64_tr_b16 %0, %2\n\tds_read_b64_tr_b16 %1, %3\n\ts_waitcnt lgkmcnt(0)"
                     : "=&v"(lo), "=&v"(hi)
                     : "v"(a0), "v"(a1)
                     : "memory");
        bfr[n] = (bf16x8){lo[0], lo[1], lo[2], lo[3], hi[0], hi[1], hi[2], hi[3]};
      }
    }
#pragma unroll
    for (int m = 0; m < 4; ++m)
#pragma unroll
      for (int n = 0; n < 4; ++n)
        acc[m][n] = __builtin_amdgcn_mfma_f32_16x16x32_bf16(af[m], bfr[n], acc[m][n], 0, 0, 0);
    if (kt + 1 < nk) swrite(buf ^ 1);
    __syncthreads();
  }
#pragma unroll
  for (int m = 0; m < 4; ++m)
#pragma unroll
    for (int n = 0; n < 4; ++n)
#pragma unroll
      for (int j = 0; j < 4; ++j)
        C[(size_t)(row0 + wr * 64 + m * 16 + (lane >> 4) * 4 + j) * ldc + col0 + wc * 64 + n * 16 + (lane & 15)] =
            f2bf(acc[m][n][j]);
}

__global__ __launch_bounds__(256, 2) void attnA(const u16* __restrict__ qkv, u16* __restrict__ o) {
  const int h = blockIdx.x % A_H, qb = blockIdx.x / A_H;
  const int tid = threadIdx.x, lane = tid & 63, wid = tid >> 6;
  const int q0 = qb * 64;
  const int kbase = q0 - 256;
  __shared__ __attribute__((aligned(16))) u16 sK[320 * 32];
  __shared__ __attribute__((aligned(16))) u16 sV[320 * 32];
  __shared__ __attribute__((aligned(16))) u16 sP[4][16 * 296];
  for (int c = tid; c < 320 * 4; c += 256) {
    int r = c >> 2, ch = c & 3;
    int key = kbase + r;
    u32x4 kv = (u32x4){0, 0, 0, 0}, vv = (u32x4){0, 0, 0, 0};
    if (key >= 0) {
      kv = *reinterpret_cast<const u32x4*>(qkv + (size_t)key * QKVA_LD + D_MODEL + h * 32 + ch * 8);
      vv = *reinterpret_cast<const u32x4*>(qkv + (size_t)key * QKVA_LD + 2 * D_MODEL + h * 32 + ch * 8);
    }
    *reinterpret_cast<u32x4*>(sK + r * 32 + ch * 8) = kv;
    *reinterpret_cast<u32x4*>(sV + r * 32 + ch * 8) = vv;
  }
  __syncthreads();
  const int qr0 = q0 + wid * 16;
  bf16x8 qf = *reinterpret_cast<const bf16x8*>(qkv + (size_t)(qr0 + (lane & 15)) * QKVA_LD + h * 32 + (lane >> 4) * 8);
  const int t0 = wid;
  f32x4 sc[17];
  const float scale = 0.17677669529663687f;
  float mx[4] = {-3e38f, -3e38f, -3e38f, -3e38f};
#pragma unroll
  for (int t = 0; t < 17; ++t) {
    bf16x8 kf = *reinterpret_cast<const bf16x8*>(sK + ((t0 + t) * 16 + (lane & 15)) * 32 + (lane >> 4) * 8);
    f32x4 s = __builtin_amdgcn_mfma_f32_16x16x32_bf16(qf, kf, (f32x4){0.f, 0.f, 0.f, 0.f}, 0, 0, 0);
    const int key = kbase + (t0 + t) * 16 + (lane & 15);
#pragma unroll
    for (int j = 0; j < 4; ++j) {
      const int row = qr0 + (lane >> 4) * 4 + j;
      float v = rbf(rbf(s[j]) * scale);
      const bool ok = (key <= row) && (row - key < A_W) && (key >= 0);
      v = ok ? v : -1e9f;
      s[j] = v;
      mx[j] = fmaxf(mx[j], v);
    }
    sc[t] = s;
  }
#pragma unroll
  for (int j = 0; j < 4; ++j) {
#pragma unroll
    for (int m = 1; m < 16; m <<= 1) mx[j] = fmaxf(mx[j], __shfl_xor(mx[j], m));
  }
  float sum[4] = {0.f, 0.f, 0.f, 0.f};
#pragma unroll
  for (int t = 0; t < 17; ++t)
#pragma unroll
    for (int j = 0; j < 4; ++j) {
      float e = __expf(sc[t][j] - mx[j]);
      sc[t][j] = e;
      sum[j] += e;
    }
#pragma unroll
  for (int j = 0; j < 4; ++j) {
#pragma unroll
    for (int m = 1; m < 16; m <<= 1) sum[j] += __shfl_xor(sum[j], m);
    sum[j] = 1.f / sum[j];
  }
  u16* P = sP[wid];
#pragma unroll
  for (int t = 0; t < 17; ++t)
#pragma unroll
    for (int j = 0; j < 4; ++j) P[((lane >> 4) * 4 + j) * 296 + t * 16 + (lane & 15)] = f2bf(sc[t][j] * sum[j]);
#pragma unroll
  for (int j = 0; j < 4; ++j) P[((lane >> 4) * 4 + j) * 296 + 272 + (lane & 15)] = 0;
  __syncthreads();
  f32x4 oacc[2] = {(f32x4){0.f, 0.f, 0.f, 0.f}, (f32x4){0.f, 0.f, 0.f, 0.f}};
  for (int ks = 0; ks < 9; ++ks) {
    bf16x8 pf = *reinterpret_cast<const bf16x8*>(P + (lane & 15) * 296 + ks * 32 + (lane >> 4) * 8);
#pragma unroll
    for (int n = 0; n < 2; ++n) {
      bf16x8 vf;
#pragma unroll
      for (int j = 0; j < 8; ++j) {
        int kl = t0 * 16 + ks * 32 + (lane >> 4) * 8 + j;
        kl = kl < 320 ? kl : 319;
        vf[j] = (short)sV[kl * 32 + n * 16 + (lane & 15)];
      }
      oacc[n] = __builtin_amdgcn_mfma_f32_16x16x32_bf16(pf, vf, oacc[n], 0, 0, 0);
    }
  }
#pragma unroll
  for (int n = 0; n < 2; ++n)
#pragma unroll
    for (int j = 0; j < 4; ++j)
      o[(size_t)(qr0 + (lane >> 4) * 4 + j) * D_MODEL + h * 32 + n * 16 + (lane & 15)] = f2bf(oacc[n][j]);
}

__global__ __launch_bounds__(256, 2) void attnBx(const u16* __restrict__ qkv, const u16* __restrict__ kvm,
                                                 u16* __restrict__ o) {
  const int seg = blockIdx.x / 8, hg = blockIdx.x % 8;
  const int tid = threadIdx.x, lane = tid & 63, wid = tid >> 6;
  const int h = hg * 4 + wid;
  __shared__ __attribute__((aligned(16))) u16 sK[4][48 * 136];
  __shared__ __attribute__((aligned(16))) u16 sV[4][64 * 136];
  __shared__ __attribute__((aligned(16))) u16 sP[4][32 * 72];
  u16* K = sK[wid];
  u16* V = sV[wid];
  u16* P = sP[wid];
  for (int c = lane; c < 64 * 16; c += 64) {
    int r = c >> 4, ch = c & 15;
    u32x4 kv = (u32x4){0, 0, 0, 0}, vv = (u32x4){0, 0, 0, 0};
    if (r < 8) {
      kv = *reinterpret_cast<const u32x4*>(kvm + ((size_t)(seg * 2 + 0) * 8 + r) * B_HD + h * 128 + ch * 8);
      vv = *reinterpret_cast<const u32x4*>(kvm + ((size_t)(seg * 2 + 1) * 8 + r) * B_HD + h * 128 + ch * 8);
    } else if (r < 40) {
      const u16* rowp = qkv + (size_t)(seg * 32 + r - 8) * QKVB_LD;
      kv = *reinterpret_cast<const u32x4*>(rowp + B_HD + h * 128 + ch * 8);
      vv = *reinterpret_cast<const u32x4*>(rowp + 2 * B_HD + h * 128 + ch * 8);
    }
    if (r < 48) *reinterpret_cast<u32x4*>(K + r * 136 + ch * 8) = kv;
    *reinterpret_cast<u32x4*>(V + r * 136 + ch * 8) = vv;
  }
  __syncthreads();
  const float scale = 0.08838834764831845f;
#pragma unroll
  for (int mt = 0; mt < 2; ++mt) {
    bf16x8 qf[4];
#pragma unroll
    for (int ks = 0; ks < 4; ++ks)
      qf[ks] = *reinterpret_cast<const bf16x8*>(qkv + (size_t)(seg * 32 + mt * 16 + (lane & 15)) * QKVB_LD + h * 128 +
                                                ks * 32 + (lane >> 4) * 8);
    f32x4 sc[3];
    float mx[4] = {-3e38f, -3e38f, -3e38f, -3e38f};
#pragma unroll
    for (int t = 0; t < 3; ++t) {
      f32x4 s = (f32x4){0.f, 0.f, 0.f, 0.f};
#pragma unroll
      for (int ks = 0; ks < 4; ++ks) {
        bf16x8 kf = *reinterpret_cast<const bf16x8*>(K + (t * 16 + (lane & 15)) * 136 + ks * 32 + (lane >> 4) * 8);
        s = __builtin_amdgcn_mfma_f32_16x16x32_bf16(qf[ks], kf, s, 0, 0, 0);
      }
      const int key = t * 16 + (lane & 15);
#pragma unroll
      for (int j = 0; j < 4; ++j) {
        const int rowp = 8 + mt * 16 + (lane >> 4) * 4 + j;
        float v = rbf(rbf(s[j]) * scale);
        const bool ok = (key <= rowp);
        v = ok ? v : -1e9f;
        s[j] = v;
        mx[j] = fmaxf(mx[j], v);
      }
      sc[t] = s;
    }
#pragma unroll
    for (int j = 0; j < 4; ++j) {
#pragma unroll
      for (int m = 1; m < 16; m <<= 1) mx[j] = fmaxf(mx[j], __shfl_xor(mx[j], m));
    }
    float sum[4] = {0.f, 0.f, 0.f, 0.f};
#pragma unroll
    for (int t = 0; t < 3; ++t)
#pragma unroll
      for (int j = 0; j < 4; ++j) {
        float e = __expf(sc[t][j] - mx[j]);
        sc[t][j] = e;
        sum[j] += e;
      }
#pragma unroll
    for (int j = 0; j < 4; ++j) {
#pragma unroll
      for (int m = 1; m < 16; m <<= 1) sum[j] += __shfl_xor(sum[j], m);
      sum[j] = 1.f / sum[j];
    }
#pragma unroll
    for (int t = 0; t < 3; ++t)
#pragma unroll
      for (int j = 0; j < 4; ++j)
        P[(mt * 16 + (lane >> 4) * 4 + j) * 72 + t * 16 + (lane & 15)] = f2bf(sc[t][j] * sum[j]);
#pragma unroll
    for (int j = 0; j < 4; ++j) P[(mt * 16 + (lane >> 4) * 4 + j) * 72 + 48 + (lane & 15)] = 0;
  }
  __syncthreads();
#pragma unroll
  for (int mt = 0; mt < 2; ++mt) {
    bf16x8 pf[2];
#pragma unroll
    for (int ks = 0; ks < 2; ++ks)
      pf[ks] = *reinterpret_cast<const bf16x8*>(P + (mt * 16 + (lane & 15)) * 72 + ks * 32 + (lane >> 4) * 8);
#pragma unroll
    for (int n = 0; n < 8; ++n) {
      f32x4 oacc = (f32x4){0.f, 0.f, 0.f, 0.f};
#pragma unroll
      for (int ks = 0; ks < 2; ++ks) {
        bf16x8 vf;
#pragma unroll
        for (int j = 0; j < 8; ++j) vf[j] = (short)V[(ks * 32 + (lane >> 4) * 8 + j) * 136 + n * 16 + (lane & 15)];
        oacc = __builtin_amdgcn_mfma_f32_16x16x32_bf16(pf[ks], vf, oacc, 0, 0, 0);
      }
#pragma unroll
      for (int j = 0; j < 4; ++j)
        o[(size_t)(seg * 32 + mt * 16 + (lane >> 4) * 4 + j) * B_HD + h * 128 + n * 16 + (lane & 15)] = f2bf(oacc[j]);
    }
  }
}

constexpr unsigned kSpinCap = 1u << 22;

__device__ __forceinline__ bool spin_fail(unsigned& spins, gu32* tmo, int lane) {
  __builtin_amdgcn_s_sleep(1);
  if (++spins > kSpinCap) {
    if (lane == 0) __hip_atomic_store(tmo, 1u, RLX_AGENT);
    return true;
  }
  return (spins & 255u) == 0u && __builtin_amdgcn_readfirstlane(__hip_atomic_load(tmo, RLX_AGENT));
}
__device__ __forceinline__ void store_granule(gu64* g, unsigned epoch, unsigned value) {
  __hip_atomic_store(g, ((unsigned long long)epoch << 32) | value, RLX_AGENT);
}
template <int N>
__device__ __forceinline__ bool sweep_granules(gu64* g, int S, unsigned epoch, unsigned (&v)[N], gu32* tmo, int lane) {
  for (unsigned spins = 0;;) {
    bool ok = true;
#pragma unroll
    for (int k = 0; k < N; ++k) {
      const unsigned long long x = __hip_atomic_load(g + k * S, RLX_AGENT);
      v[k] = (unsigned)x;
      ok &= (x >> 32) == epoch;
    }
    if (__all(ok)) return true;
    if (spin_fail(spins, tmo, lane)) return false;
  }
}

struct ChainArgs {
  const u16* mem0;
  const u16* Wq;
  const u16* Wk;
  const u16* Wv;
  const u16* Wo;
  const u16* qkvB;
  u16* kvm;
  unsigned long long* gMem;
  unsigned long long* gQK;
  unsigned long long* gO;
  unsigned* tmo;
};

#define STAMP(idx) do {} while (0)
constexpr int MEM_LDS_STRIDE = 1032;

__global__ __launch_bounds__(256, 1) void chain_kernel(ChainArgs a) {
  extern __shared__ __attribute__((aligned(16))) char lds[];
  const int b = blockIdx.x;
  const int tid = threadIdx.x, lane = tid & 63;
  const int w = __builtin_amdgcn_readfirstlane(tid >> 6);
  gu64* gMem = (gu64*)a.gMem;
  gu64* gQK = (gu64*)a.gQK;
  gu64* gO = (gu64*)a.gO;
  gu32* tmo = (gu32*)a.tmo;

  if (b < 192) {
    const int t = b / 64, cb = b % 64;
    const u16* W = t == 0 ? a.Wq : (t == 1 ? a.Wk : a.Wv);
    const int col = cb * 64 + w * 16 + (lane & 15);
    bf16x8 wf[32];
#pragma unroll
    for (int i = 0; i < 32; ++i) {
#pragma unroll
      for (int j = 0; j < 8; ++j) wf[i][j] = (short)W[(size_t)(i * 32 + (lane >> 4) * 8 + j) * B_HD + col];
      asm volatile("" : "+v"(wf[i]));
    }
    u16* sMem = (u16*)lds;
    u16* sQ = (u16*)(lds + 16896);
    u16* sK = sQ + 8 * 136;
    u16* sVT = sK + 48 * 136;
    if (t == 2) {
      for (int i = tid; i < 8 * 136; i += 256) sK[40 * 136 + i] = 0;
      for (int i = tid; i < 64 * 8; i += 256) sVT[(i >> 3) * 56 + 40 + (i & 7)] = 0;
    }
    const int h = cb >> 1, half = cb & 1;
    for (int s = 0; s < B_NSEG; ++s) {
      const unsigned epoch = (unsigned)s + 1u;
      u32x4 kxr0 = (u32x4){0, 0, 0, 0}, kxr1 = (u32x4){0, 0, 0, 0}, vxr = (u32x4){0, 0, 0, 0};
      if (t == 2) {
        {
          int r = tid >> 4, ch = tid & 15;
          kxr0 = *reinterpret_cast<const u32x4*>(a.qkvB + (size_t)(s * 32 + r) * QKVB_LD + B_HD + h * 128 + ch * 8);
          kxr1 = *reinterpret_cast<const u32x4*>(a.qkvB + (size_t)(s * 32 + 16 + r) * QKVB_LD + B_HD + h * 128 + ch * 8);
        }
        {
          int r = tid >> 3, ch = tid & 7;
          vxr = *reinterpret_cast<const u32x4*>(a.qkvB + (size_t)(s * 32 + r) * QKVB_LD + 2 * B_HD + h * 128 +
                                                half * 64 + ch * 8);
        }
      }
      STAMP(0);
      if (s == 0) {
#pragma unroll
        for (int k = 0; k < 16; ++k) {
          int g = tid + 256 * k;
          int r = g >> 9, cp = g & 511;
          unsigned v = *reinterpret_cast<const unsigned*>(a.mem0 + r * 1024 + cp * 2);
          *reinterpret_cast<unsigned*>(sMem + r * MEM_LDS_STRIDE + cp * 2) = v;
        }
      } else {
        unsigned v[16];
        {
          unsigned spins = 0;
          for (;;) {
            const unsigned long long x = __hip_atomic_load(gMem + 7 * 512 + lane * 8 + 7, RLX_AGENT);
            if (__all((x >> 32) == (unsigned)s)) break;
            if (spin_fail(spins, tmo, lane)) return;
          }
        }
        if (!sweep_granules<16>(gMem + tid, 256, (unsigned)s, v, tmo, lane)) return;
#pragma unroll
        for (int k = 0; k < 16; ++k) {
          int g = tid + 256 * k;
          int r = g >> 9, cp = g & 511;
          *reinterpret_cast<unsigned*>(sMem + r * MEM_LDS_STRIDE + cp * 2) = v[k];
        }
      }
      __syncthreads();
      STAMP(1);
      f32x4 acc = (f32x4){0.f, 0.f, 0.f, 0.f}, acc2 = (f32x4){0.f, 0.f, 0.f, 0.f};
      {
        bf16x8 af[32];
#pragma unroll
        for (int i = 0; i < 32; ++i)
          af[i] = *reinterpret_cast<const bf16x8*>(sMem + (lane & 7) * MEM_LDS_STRIDE + i * 32 + (lane >> 4) * 8);
#pragma unroll
        for (int i = 0; i < 32; i += 2) {
          acc = __builtin_amdgcn_mfma_f32_16x16x32_bf16(wf[i], af[i], acc, 0, 0, 0);
          acc2 = __builtin_amdgcn_mfma_f32_16x16x32_bf16(wf[i + 1], af[i + 1], acc2, 0, 0, 0);
        }
        acc += acc2;
      }
      {
        if ((lane & 15) < 8) {
          const int r = lane & 15;
#pragma unroll
          for (int jj = 0; jj < 2; ++jj) {
            const unsigned pk = (unsigned)f2bf(acc[2 * jj]) | ((unsigned)f2bf(acc[2 * jj + 1]) << 16);
            const int cp = w * 8 + (lane >> 4) * 2 + jj;
            if (t < 2) store_granule(gQK + (size_t)t * 16384 + r * 2048 + cb * 32 + cp, epoch, pk);
            if (t >= 1)
              *reinterpret_cast<unsigned*>(a.kvm + ((size_t)(s * 2 + (t - 1)) * 8 + r) * B_HD + cb * 64 + cp * 2) = pk;
            if (t == 2) {
              sVT[(cp * 2) * 56 + r] = (u16)(pk & 0xffff);
              sVT[(cp * 2 + 1) * 56 + r] = (u16)(pk >> 16);
            }
          }
        }
      }
      STAMP(2);
      if (t == 2) {
        {
          int r = tid >> 4, ch = tid & 15;
          *reinterpret_cast<u32x4*>(sK + (8 + r) * 136 + ch * 8) = kxr0;
          *reinterpret_cast<u32x4*>(sK + (24 + r) * 136 + ch * 8) = kxr1;
        }
        {
          int r = tid >> 3, ch = tid & 7;
#pragma unroll
          for (int e = 0; e < 4; ++e) {
            sVT[(ch * 8 + 2 * e) * 56 + 8 + r] = (u16)(vxr[e] & 0xffff);
            sVT[(ch * 8 + 2 * e + 1) * 56 + 8 + r] = (u16)(vxr[e] >> 16);
          }
        }
        {
          unsigned v[4];
          int r = tid >> 5, cp = tid & 31;
          {
            unsigned spins = 0;
            for (;;) {
              bool ok = true;
#pragma unroll
              for (int k = 0; k < 4; ++k) {
                const unsigned long long x = __hip_atomic_load(
                    gQK + (size_t)(k >> 1) * 16384 + r * 2048 + h * 64 + (k & 1) * 32 + cp, RLX_AGENT);
                v[k] = (unsigned)x;
                ok &= (x >> 32) == epoch;
              }
              if (__all(ok)) break;
              if (spin_fail(spins, tmo, lane)) return;
            }
          }
          *reinterpret_cast<unsigned*>(sQ + r * 136 + cp * 2) = v[0];
          *reinterpret_cast<unsigned*>(sQ + r * 136 + 64 + cp * 2) = v[1];
          *reinterpret_cast<unsigned*>(sK + r * 136 + cp * 2) = v[2];
          *reinterpret_cast<unsigned*>(sK + r * 136 + 64 + cp * 2) = v[3];
        }
        __syncthreads();
        STAMP(3);
        if (w == 0) {
          bf16x8 qf[4], kf[3][4];
#pragma unroll
          for (int ks = 0; ks < 4; ++ks)
            qf[ks] = *reinterpret_cast<const bf16x8*>(sQ + (lane & 7) * 136 + ks * 32 + (lane >> 4) * 8);
#pragma unroll
          for (int tt = 0; tt < 3; ++tt)
#pragma unroll
            for (int ks = 0; ks < 4; ++ks)
              kf[tt][ks] = *reinterpret_cast<const bf16x8*>(sK + (tt * 16 + (lane & 15)) * 136 + ks * 32 + (lane >> 4) * 8);
          bf16x4 vlo[4], vmid[4], vhi[4];
#pragma unroll
          for (int n = 0; n < 4; ++n) {
            const u16* vp = sVT + (n * 16 + (lane & 15)) * 56 + (lane >> 4) * 4;
            vlo[n] = *reinterpret_cast<const bf16x4*>(vp);
            vmid[n] = *reinterpret_cast<const bf16x4*>(vp + 16);
            vhi[n] = *reinterpret_cast<const bf16x4*>(vp + 32);
          }
          f32x4 sc[3];
          float mx = -3e38f;
#pragma unroll
          for (int tt = 0; tt < 3; ++tt) {
            f32x4 sv = (f32x4){0.f, 0.f, 0.f, 0.f};
#pragma unroll
            for (int ks = 0; ks < 4; ++ks) sv = __builtin_amdgcn_mfma_f32_16x16x32_bf16(kf[tt][ks], qf[ks], sv, 0, 0, 0);
#pragma unroll
            for (int j = 0; j < 4; ++j) {
              const int key = tt * 16 + (lane >> 4) * 4 + j;
              float v = rbf(rbf(sv[j]) * 0.08838834764831845f);
              v = key < 40 ? v : -3e38f;
              sv[j] = v;
              mx = fmaxf(mx, v);
            }
            sc[tt] = sv;
          }
          mx = fmaxf(mx, __shfl_xor(mx, 16));
          mx = fmaxf(mx, __shfl_xor(mx, 32));
          float sum = 0.f;
#pragma unroll
          for (int tt = 0; tt < 3; ++tt)
#pragma unroll
            for (int j = 0; j < 4; ++j) {
              float e = __expf(sc[tt][j] - mx);
              sc[tt][j] = e;
              sum += e;
            }
          sum += __shfl_xor(sum, 16);
          sum += __shfl_xor(sum, 32);
          const float inv = 1.f / sum;
          bf16x8 p0, p1;
#pragma unroll
          for (int j = 0; j < 4; ++j) {
            p0[j] = (short)f2bf(sc[0][j] * inv);
            p0[4 + j] = (short)f2bf(sc[1][j] * inv);
            p1[j] = (short)f2bf(sc[2][j] * inv);
            p1[4 + j] = 0;
          }
#pragma unroll
          for (int n = 0; n < 4; ++n) {
            bf16x8 va = (bf16x8){vlo[n][0], vlo[n][1], vlo[n][2], vlo[n][3], vmid[n][0], vmid[n][1], vmid[n][2], vmid[n][3]};
            bf16x8 vb = (bf16x8){vhi[n][0], vhi[n][1], vhi[n][2], vhi[n][3], 0, 0, 0, 0};
            f32x4 oacc = __builtin_amdgcn_mfma_f32_16x16x32_bf16(va, p0, (f32x4){0.f, 0.f, 0.f, 0.f}, 0, 0, 0);
            oacc = __builtin_amdgcn_mfma_f32_16x16x32_bf16(vb, p1, oacc, 0, 0, 0);
            if ((lane & 15) < 8) {
#pragma unroll
              for (int jj = 0; jj < 2; ++jj)
                store_granule(gO + (lane & 15) * 2048 + cb * 32 + n * 8 + (lane >> 4) * 2 + jj, epoch,
                              (unsigned)f2bf(oacc[2 * jj]) | ((unsigned)f2bf(oacc[2 * jj + 1]) << 16));
            }
          }
          STAMP(4);
        }
      }
      __syncthreads();
    }
  } else {
    const int ob = b - 192;
    const int col = ob * 16 + (lane & 15);
    bf16x8 wf[32];
#pragma unroll
    for (int i = 0; i < 32; ++i) {
#pragma unroll
      for (int j = 0; j < 8; ++j)
        wf[i][j] = (short)a.Wo[(size_t)(w * 1024 + i * 32 + (lane >> 4) * 8 + j) * D_MODEL + col];
      asm volatile("" : "+v"(wf[i]));
    }
    u16* sO = (u16*)lds + w * (8 * 1032);
    float* sRed = (float*)(lds + 4 * 8 * 1032 * 2);
    for (int s = 0; s < B_NSEG; ++s) {
      const unsigned epoch = (unsigned)s + 1u;
      STAMP(0);
      {
        unsigned spins = 0;
        for (;;) {
          const unsigned long long x = __hip_atomic_load(gO + 7 * 2048 + w * 512 + (lane & 15) * 32 + 31, RLX_AGENT);
          bool ok = (x >> 32) == epoch;
          if (__all(ok)) break;
          if (spin_fail(spins, tmo, lane)) return;
        }
      }
#pragma unroll
      for (int hb = 0; hb < 2; ++hb) {
        unsigned v[32];
        unsigned spins = 0;
        for (;;) {
          bool ok = true;
#pragma unroll
          for (int k = 0; k < 32; ++k) {
            const unsigned long long x =
                __hip_atomic_load(gO + (hb * 4 + (k >> 3)) * 2048 + w * 512 + (k & 7) * 64 + lane, RLX_AGENT);
            v[k] = (unsigned)x;
            ok &= (x >> 32) == epoch;
          }
          if (__all(ok)) break;
          if (spin_fail(spins, tmo, lane)) return;
        }
#pragma unroll
        for (int k = 0; k < 32; ++k)
          *reinterpret_cast<unsigned*>(sO + (hb * 4 + (k >> 3)) * 1032 + ((k & 7) * 64 + lane) * 2) = v[k];
      }
      __builtin_amdgcn_s_waitcnt(0xc07f);
      STAMP(1);
      f32x4 acc = (f32x4){0.f, 0.f, 0.f, 0.f}, acc2 = (f32x4){0.f, 0.f, 0.f, 0.f};
      {
        bf16x8 af[32];
#pragma unroll
        for (int i = 0; i < 32; ++i)
          af[i] = *reinterpret_cast<const bf16x8*>(sO + (lane & 7) * 1032 + i * 32 + (lane >> 4) * 8);
#pragma unroll
        for (int i = 0; i < 32; i += 2) {
          acc = __builtin_amdgcn_mfma_f32_16x16x32_bf16(wf[i], af[i], acc, 0, 0, 0);
          acc2 = __builtin_amdgcn_mfma_f32_16x16x32_bf16(wf[i + 1], af[i + 1], acc2, 0, 0, 0);
        }
        acc += acc2;
      }
      if ((lane & 15) < 8) {
#pragma unroll
        for (int j = 0; j < 4; ++j) sRed[(w * 8 + (lane & 15)) * 16 + (lane >> 4) * 4 + j] = acc[j];
      }
      __syncthreads();
      if (tid < 64) {
        int r = tid >> 3, cp = tid & 7;
        float v0 = 0.f, v1 = 0.f;
#pragma unroll
        for (int q = 0; q < 4; ++q) {
          v0 += sRed[(q * 8 + r) * 16 + cp * 2];
          v1 += sRed[(q * 8 + r) * 16 + cp * 2 + 1];
        }
        unsigned pk = (unsigned)f2bf(v0) | ((unsigned)f2bf(v1) << 16);
        if (s + 1 < B_NSEG) store_granule(gMem + r * 512 + ob * 8 + cp, epoch, pk);
      }
      STAMP(2);
      __syncthreads();
    }
  }
}

extern "C" void kernel_launch(void* const* d_in, const int* in_sizes, int n_in, void* d_out, int out_size, void* d_ws,
                              size_t ws_size, hipStream_t stream) {
  const u16* A_x = (const u16*)d_in[0];
  const u16* A_wqkv = (const u16*)d_in[1];
  const u16* A_wo = (const u16*)d_in[2];
  const u16* B_x = (const u16*)d_in[3];
  const u16* B_mem0 = (const u16*)d_in[4];
  const u16* B_Wq = (const u16*)d_in[5];
  const u16* B_Wk = (const u16*)d_in[6];
  const u16* B_Wv = (const u16*)d_in[7];
  const u16* B_Wo = (const u16*)d_in[8];
  u16* out = (u16*)d_out;
  char* ws = (char*)d_ws;
  u16* qkvB = (u16*)ws;
  u16* qkvA = (u16*)(ws + (size_t)50331648);
  u16* oA = (u16*)(ws + (size_t)50331648 + 12582912);
  u16* kvm = (u16*)(ws + (size_t)67108864);
  u16* oB = (u16*)(ws + (size_t)75497472);
  char* gran = ws + (size_t)92274688;
  const size_t gran_bytes = 32768 + 262144 + 131072 + 256;
  (void)hipMemsetAsync(gran, 0, gran_bytes, stream);

  gemm128<<<(2048 / 128) * (4096 / 128), 256, 0, stream>>>(B_x, 1024, B_Wq, 4096, qkvB, QKVB_LD, 2048, 4096, 1024);
  gemm128<<<(2048 / 128) * (4096 / 128), 256, 0, stream>>>(B_x, 1024, B_Wk, 4096, qkvB + 4096, QKVB_LD, 2048, 4096, 1024);
  gemm128<<<(2048 / 128) * (4096 / 128), 256, 0, stream>>>(B_x, 1024, B_Wv, 4096, qkvB + 8192, QKVB_LD, 2048, 4096, 1024);
  ChainArgs ca;
  ca.mem0 = B_mem0;
  ca.Wq = B_Wq;
  ca.Wk = B_Wk;
  ca.Wv = B_Wv;
  ca.Wo = B_Wo;
  ca.qkvB = qkvB;
  ca.kvm = kvm;
  ca.gMem = (unsigned long long*)gran;
  ca.gQK = (unsigned long long*)(gran + 32768);
  ca.gO = (unsigned long long*)(gran + 32768 + 262144);
  ca.tmo = (unsigned*)(gran + 32768 + 262144 + 131072);
  (void)hipFuncSetAttribute((const void*)chain_kernel, hipFuncAttributeMaxDynamicSharedMemorySize, 96 * 1024);
  chain_kernel<<<256, 256, 96 * 1024, stream>>>(ca);
  attnBx<<<B_NSEG * 8, 256, 0, stream>>>(qkvB, kvm, oB);
  gemm128<<<(2048 / 128) * (1024 / 128), 256, 0, stream>>>(oB, 4096, B_Wo, 1024, out + 1024, OUT_LD, 2048, 1024, 4096);
  gemm128<<<(2048 / 128) * (3072 / 128), 256, 0, stream>>>(A_x, 1024, A_wqkv, 3072, qkvA, QKVA_LD, 2048, 3072, 1024);
  attnA<<<(2048 / 64) * A_H, 256, 0, stream>>>(qkvA, oA);
  gemm128<<<(2048 / 128) * (1024 / 128), 256, 0, stream>>>(oA, 1024, A_wo, 1024, out, OUT_LD, 2048, 1024, 1024);
}
```

```cpp
#include <hip/hip_runtime.h>
#include <hip/hip_bf16.h>
#include <stdint.h>

constexpr int S_LEN = 2048;
constexpr int D_MODEL = 1024;
constexpr int A_H = 32, A_DH = 32, A_W = 256;
constexpr int B_H = 32, B_DH = 128, B_M = 8, B_SEG = 32, B_NSEG = 64;
constexpr int B_HD = B_H * B_DH;
constexpr int QKVB_LD = 3 * B_HD;
constexpr int QKVA_LD = 3 * D_MODEL;
constexpr int OUT_LD = 2 * D_MODEL;

typedef unsigned short u16;
typedef __attribute__((ext_vector_type(8))) short bf16x8;
typedef __attribute__((ext_vector_type(4))) short bf16x4;
typedef __attribute__((ext_vector_type(4))) float f32x4;
typedef __attribute__((ext_vector_type(4))) unsigned u32x4;
#define GAS __attribute__((address_space(1)))
typedef GAS unsigned gu32;
typedef GAS unsigned long long gu64;
#define RLX_AGENT __ATOMIC_RELAXED, __HIP_MEMORY_SCOPE_AGENT

__device__ __forceinline__ float bf2f(u16 v) { return __uint_as_float(((unsigned)v) << 16); }
__device__ __forceinline__ u16 f2bf(float f) {
  __hip_bfloat16 b = __float2bfloat16(f);
  return *reinterpret_cast<u16*>(&b);
}
__device__ __forceinline__ float rbf(float f) { return bf2f(f2bf(f)); }

__device__ __forceinline__ unsigned boff(unsigned row, unsigned ch) {
  return 256u * row + 16u * (ch ^ (((row & 3) << 2) | ((row >> 2) & 3)));
}

template <int MT>
__global__ __launch_bounds__(256, 2) void gemm128(const u16* __restrict__ A, int lda,
                                                  const u16* __restrict__ B, int ldb,
                                                  u16* __restrict__ C, int ldc, int M, int N, int K) {
  constexpr int BM = MT * 32;
  __shared__ __attribute__((aligned(16))) char smem[2 * (8192 + 8192)];
  const int nbn = N / 128;
  const int bm = blockIdx.x / nbn, bn = blockIdx.x % nbn;
  const int tid = threadIdx.x, lane = tid & 63, wid = tid >> 6;
  const int wr = wid >> 1, wc = wid & 1;
  const int row0 = bm * BM, col0 = bn * 128;
  u32x4 ra[2], rb[2];
  auto gload = [&](int kt) {
#pragma unroll
    for (int i = 0; i < 2; ++i) {
      int c = tid + 256 * i;
      int r = c >> 2, ch = c & 3;
      if (r < BM) ra[i] = *reinterpret_cast<const u32x4*>(A + (size_t)(row0 + r) * lda + kt * 32 + ch * 8);
      int kr = c >> 4, chb = c & 15;
      rb[i] = *reinterpret_cast<const u32x4*>(B + (size_t)(kt * 32 + kr) * ldb + col0 + chb * 8);
    }
  };
  auto swrite = [&](int buf) {
    char* sa = smem + buf * 16384;
    char* sb = sa + 8192;
#pragma unroll
    for (int i = 0; i < 2; ++i) {
      int c = tid + 256 * i;
      int r = c >> 2, ch = c & 3;
      if (r < BM) *reinterpret_cast<u32x4*>(sa + r * 64 + ch * 16) = ra[i];
      int kr = c >> 4, chb = c & 15;
      *reinterpret_cast<u32x4*>(sb + boff(kr, chb)) = rb[i];
    }
  };
  f32x4 acc[MT][4];
#pragma unroll
  for (int m = 0; m < MT; ++m)
#pragma unroll
    for (int n = 0; n < 4; ++n) acc[m][n] = (f32x4){0.f, 0.f, 0.f, 0.f};
  const int nk = K / 32;
  gload(0);
  swrite(0);
  __syncthreads();
  for (int kt = 0; kt < nk; ++kt) {
    const int buf = kt & 1;
    if (kt + 1 < nk) gload(kt + 1);
    const char* sa = smem + buf * 16384;
    const char* sb = sa + 8192;
    bf16x8 af[MT], bfr[4];
#pragma unroll
    for (int m = 0; m < MT; ++m)
      af[m] = *reinterpret_cast<const bf16x8*>(sa + (wr * (MT * 16) + m * 16 + (lane & 15)) * 64 + (lane >> 4) * 16);
    {
      const unsigned g = lane >> 4, q = (lane & 15) >> 2, p = lane & 3;
      const unsigned base = (unsigned)(size_t)sb;
#pragma unroll
      for (int n = 0; n < 4; ++n) {
        const unsigned c = wc * 4 + n;
        unsigned a0 = base + boff(8 * g + 0 + q, 2 * c + (p >> 1)) + 8 * (p & 1);
        unsigned a1 = base + boff(8 * g + 4 + q, 2 * c + (p >> 1)) + 8 * (p & 1);
        bf16x4 lo, hi;
        asm volatile("ds_read_b64_tr_b16 %0, %2\n\tds_read_b64_tr_b16 %1, %3\n\ts_waitcnt lgkmcnt(0)"
                     : "=&v"(lo), "=&v"(hi)
                     : "v"(a0), "v"(a1)
                     : "memory");
        bfr[n] = (bf16x8){lo[0], lo[1], lo[2], lo[3], hi[0], hi[1], hi[2], hi[3]};
      }
    }
#pragma unroll
    for (int m = 0; m < MT; ++m)
#pragma unroll
      for (int n = 0; n < 4; ++n)
        acc[m][n] = __builtin_amdgcn_mfma_f32_16x16x32_bf16(af[m], bfr[n], acc[m][n], 0, 0, 0);
    if (kt + 1 < nk) swrite(buf ^ 1);
    __syncthreads();
  }
#pragma unroll
  for (int m = 0; m < MT; ++m)
#pragma unroll
    for (int n = 0; n < 4; ++n)
#pragma unroll
      for (int j = 0; j < 4; ++j)
        C[(size_t)(row0 + wr * (MT * 16) + m * 16 + (lane >> 4) * 4 + j) * ldc + col0 + wc * 64 + n * 16 + (lane & 15)] =
            f2bf(acc[m][n][j]);
}

__global__ __launch_bounds__(256, 2) void attnA(const u16* __restrict__ qkv, u16* __restrict__ o) {
  const int h = blockIdx.x % A_H, qb = blockIdx.x / A_H;
  const int tid = threadIdx.x, lane = tid & 63, wid = tid >> 6;
  const int q0 = qb * 64;
  const int kbase = q0 - 256;
  __shared__ __attribute__((aligned(16))) u16 sK[320 * 32];
  __shared__ __attribute__((aligned(16))) u16 sV[320 * 32];
  __shared__ __attribute__((aligned(16))) u16 sP[4][16 * 296];
  for (int c = tid; c < 320 * 4; c += 256) {
    int r = c >> 2, ch = c & 3;
    int key = kbase + r;
    u32x4 kv = (u32x4){0, 0, 0, 0}, vv = (u32x4){0, 0, 0, 0};
    if (key >= 0) {
      kv = *reinterpret_cast<const u32x4*>(qkv + (size_t)key * QKVA_LD + D_MODEL + h * 32 + ch * 8);
      vv = *reinterpret_cast<const u32x4*>(qkv + (size_t)key * QKVA_LD + 2 * D_MODEL + h * 32 + ch * 8);
    }
    *reinterpret_cast<u32x4*>(sK + r * 32 + ch * 8) = kv;
    *reinterpret_cast<u32x4*>(sV + r * 32 + ch * 8) = vv;
  }
  __syncthreads();
  const int qr0 = q0 + wid * 16;
  bf16x8 qf = *reinterpret_cast<const bf16x8*>(qkv + (size_t)(qr0 + (lane & 15)) * QKVA_LD + h * 32 + (lane >> 4) * 8);
  const int t0 = wid;
  f32x4 sc[17];
  const float scale = 0.17677669529663687f;
  float mx[4] = {-3e38f, -3e38f, -3e38f, -3e38f};
#pragma unroll
  for (int t = 0; t < 17; ++t) {
    bf16x8 kf = *reinterpret_cast<const bf16x8*>(sK + ((t0 + t) * 16 + (lane & 15)) * 32 + (lane >> 4) * 8);
    f32x4 s = __builtin_amdgcn_mfma_f32_16x16x32_bf16(qf, kf, (f32x4){0.f, 0.f, 0.f, 0.f}, 0, 0, 0);
    const int key = kbase + (t0 + t) * 16 + (lane & 15);
#pragma unroll
    for (int j = 0; j < 4; ++j) {
      const int row = qr0 + (lane >> 4) * 4 + j;
      float v = rbf(rbf(s[j]) * scale);
      const bool ok = (key <= row) && (row - key < A_W) && (key >= 0);
      v = ok ? v : -1e9f;
      s[j] = v;
      mx[j] = fmaxf(mx[j], v);
    }
    sc[t] = s;
  }
#pragma unroll
  for (int j = 0; j < 4; ++j) {
#pragma unroll
    for (int m = 1; m < 16; m <<= 1) mx[j] = fmaxf(mx[j], __shfl_xor(mx[j], m));
  }
  float sum[4] = {0.f, 0.f, 0.f, 0.f};
#pragma unroll
  for (int t = 0; t < 17; ++t)
#pragma unroll
    for (int j = 0; j < 4; ++j) {
      float e = __expf(sc[t][j] - mx[j]);
      sc[t][j] = e;
      sum[j] += e;
    }
#pragma unroll
  for (int j = 0; j < 4; ++j) {
#pragma unroll
    for (int m = 1; m < 16; m <<= 1) sum[j] += __shfl_xor(sum[j], m);
    sum[j] = 1.f / sum[j];
  }
  u16* P = sP[wid];
#pragma unroll
  for (int t = 0; t < 17; ++t)
#pragma unroll
    for (int j = 0; j < 4; ++j) P[((lane >> 4) * 4 + j) * 296 + t * 16 + (lane & 15)] = f2bf(sc[t][j] * sum[j]);
#pragma unroll
  for (int j = 0; j < 4; ++j) P[((lane >> 4) * 4 + j) * 296 + 272 + (lane & 15)] = 0;
  __syncthreads();
  f32x4 oacc[2] = {(f32x4){0.f, 0.f, 0.f, 0.f}, (f32x4){0.f, 0.f, 0.f, 0.f}};
  for (int ks = 0; ks < 9; ++ks) {
    bf16x8 pf = *reinterpret_cast<const bf16x8*>(P + (lane & 15) * 296 + ks * 32 + (lane >> 4) * 8);
#pragma unroll
    for (int n = 0; n < 2; ++n) {
      bf16x8 vf;
#pragma unroll
      for (int j = 0; j < 8; ++j) {
        int kl = t0 * 16 + ks * 32 + (lane >> 4) * 8 + j;
        kl = kl < 320 ? kl : 319;
        vf[j] = (short)sV[kl * 32 + n * 16 + (lane & 15)];
      }
      oacc[n] = __builtin_amdgcn_mfma_f32_16x16x32_bf16(pf, vf, oacc[n], 0, 0, 0);
    }
  }
#pragma unroll
  for (int n = 0; n < 2; ++n)
#pragma unroll
    for (int j = 0; j < 4; ++j)
      o[(size_t)(qr0 + (lane >> 4) * 4 + j) * D_MODEL + h * 32 + n * 16 + (lane & 15)] = f2bf(oacc[n][j]);
}

__global__ __launch_bounds__(256, 2) void attnBx(const u16* __restrict__ qkv, const u16* __restrict__ kvm,
                                                 u16* __restrict__ o) {
  const int seg = blockIdx.x / 8, hg = blockIdx.x % 8;
  const int tid = threadIdx.x, lane = tid & 63, wid = tid >> 6;
  const int h = hg * 4 + wid;
  __shared__ __attribute__((aligned(16))) u16 sK[4][48 * 136];
  __shared__ __attribute__((aligned(16))) u16 sV[4][64 * 136];
  __shared__ __attribute__((aligned(16))) u16 sP[4][32 * 72];
  u16* K = sK[wid];
  u16* V = sV[wid];
  u16* P = sP[wid];
  for (int c = lane; c < 64 * 16; c += 64) {
    int r = c >> 4, ch = c & 15;
    u32x4 kv = (u32x4){0, 0, 0, 0}, vv = (u32x4){0, 0, 0, 0};
    if (r < 8) {
      kv = *reinterpret_cast<const u32x4*>(kvm + ((size_t)(seg * 2 + 0) * 8 + r) * B_HD + h * 128 + ch * 8);
      vv = *reinterpret_cast<const u32x4*>(kvm + ((size_t)(seg * 2 + 1) * 8 + r) * B_HD + h * 128 + ch * 8);
    } else if (r < 40) {
      const u16* rowp = qkv + (size_t)(seg * 32 + r - 8) * QKVB_LD;
      kv = *reinterpret_cast<const u32x4*>(rowp + B_HD + h * 128 + ch * 8);
      vv = *reinterpret_cast<const u32x4*>(rowp + 2 * B_HD + h * 128 + ch * 8);
    }
    if (r < 48) *reinterpret_cast<u32x4*>(K + r * 136 + ch * 8) = kv;
    *reinterpret_cast<u32x4*>(V + r * 136 + ch * 8) = vv;
  }
  __syncthreads();
  const float scale = 0.08838834764831845f;
#pragma unroll
  for (int mt = 0; mt < 2; ++mt) {
    bf16x8 qf[4];
#pragma unroll
    for (int ks = 0; ks < 4; ++ks)
      qf[ks] = *reinterpret_cast<const bf16x8*>(qkv + (size_t)(seg * 32 + mt * 16 + (lane & 15)) * QKVB_LD + h * 128 +
                                                ks * 32 + (lane >> 4) * 8);
    f32x4 sc[3];
    float mx[4] = {-3e38f, -3e38f, -3e38f, -3e38f};
#pragma unroll
    for (int t = 0; t < 3; ++t) {
      f32x4 s = (f32x4){0.f, 0.f, 0.f, 0.f};
#pragma unroll
      for (int ks = 0; ks < 4; ++ks) {
        bf16x8 kf = *reinterpret_cast<const bf16x8*>(K + (t * 16 + (lane & 15)) * 136 + ks * 32 + (lane >> 4) * 8);
        s = __builtin_amdgcn_mfma_f32_16x16x32_bf16(qf[ks], kf, s, 0, 0, 0);
      }
      const int key = t * 16 + (lane & 15);
#pragma unroll
      for (int j = 0; j < 4; ++j) {
        const int rowp = 8 + mt * 16 + (lane >> 4) * 4 + j;
        float v = rbf(rbf(s[j]) * scale);
        const bool ok = (key <= rowp);
        v = ok ? v : -1e9f;
        s[j] = v;
        mx[j] = fmaxf(mx[j], v);
      }
      sc[t] = s;
    }
#pragma unroll
    for (int j = 0; j < 4; ++j) {
#pragma unroll
      for (int m = 1; m < 16; m <<= 1) mx[j] = fmaxf(mx[j], __shfl_xor(mx[j], m));
    }
    float sum[4] = {0.f, 0.f, 0.f, 0.f};
#pragma unroll
    for (int t = 0; t < 3; ++t)
#pragma unroll
      for (int j = 0; j < 4; ++j) {
        float e = __expf(sc[t][j] - mx[j]);
        sc[t][j] = e;
        sum[j] += e;
      }
#pragma unroll
    for (int j = 0; j < 4; ++j) {
#pragma unroll
      for (int m = 1; m < 16; m <<= 1) sum[j] += __shfl_xor(sum[j], m);
      sum[j] = 1.f / sum[j];
    }
#pragma unroll
    for (int t = 0; t < 3; ++t)
#pragma unroll
      for (int j = 0; j < 4; ++j)
        P[(mt * 16 + (lane >> 4) * 4 + j) * 72 + t * 16 + (lane & 15)] = f2bf(sc[t][j] * sum[j]);
#pragma unroll
    for (int j = 0; j < 4; ++j) P[(mt * 16 + (lane >> 4) * 4 + j) * 72 + 48 + (lane & 15)] = 0;
  }
  __syncthreads();
#pragma unroll
  for (int mt = 0; mt < 2; ++mt) {
    bf16x8 pf[2];
#pragma unroll
    for (int ks = 0; ks < 2; ++ks)
      pf[ks] = *reinterpret_cast<const bf16x8*>(P + (mt * 16 + (lane & 15)) * 72 + ks * 32 + (lane >> 4) * 8);
#pragma unroll
    for (int n = 0; n < 8; ++n) {
      f32x4 oacc = (f32x4){0.f, 0.f, 0.f, 0.f};
#pragma unroll
      for (int ks = 0; ks < 2; ++ks) {
        bf16x8 vf;
#pragma unroll
        for (int j = 0; j < 8; ++j) vf[j] = (short)V[(ks * 32 + (lane >> 4) * 8 + j) * 136 + n * 16 + (lane & 15)];
        oacc = __builtin_amdgcn_mfma_f32_16x16x32_bf16(pf[ks], vf, oacc, 0, 0, 0);
      }
#pragma unroll
      for (int j = 0; j < 4; ++j)
        o[(size_t)(seg * 32 + mt * 16 + (lane >> 4) * 4 + j) * B_HD + h * 128 + n * 16 + (lane & 15)] = f2bf(oacc[j]);
    }
  }
}

constexpr unsigned kSpinCap = 1u << 22;

__device__ __forceinline__ bool spin_fail(unsigned& spins, gu32* tmo, int lane) {
  __builtin_amdgcn_s_sleep(1);
  if (++spins > kSpinCap) {
    if (lane == 0) __hip_atomic_store(tmo, 1u, RLX_AGENT);
    return true;
  }
  return (spins & 255u) == 0u && __builtin_amdgcn_readfirstlane(__hip_atomic_load(tmo, RLX_AGENT));
}
__device__ __forceinline__ void store_granule(gu64* g, unsigned epoch, unsigned value) {
  __hip_atomic_store(g, ((unsigned long long)epoch << 32) | value, RLX_AGENT);
}
template <int N>
__device__ __forceinline__ bool sweep_granules(gu64* g, int S, unsigned epoch, unsigned (&v)[N], gu32* tmo, int lane) {
  for (unsigned spins = 0;;) {
    bool ok = true;
#pragma unroll
    for (int k = 0; k < N; ++k) {
      const unsigned long long x = __hip_atomic_load(g + k * S, RLX_AGENT);
      v[k] = (unsigned)x;
      ok &= (x >> 32) == epoch;
    }
    if (__all(ok)) return true;
    if (spin_fail(spins, tmo, lane)) return false;
  }
}

struct ChainArgs {
  const u16* mem0;
  const u16* Wq;
  const u16* Wk;
  const u16* Wv;
  const u16* Wo;
  const u16* qkvB;
  u16* kvm;
  unsigned long long* gMem;
  unsigned long long* gQK;
  unsigned long long* gO;
  unsigned* tmo;
};

#define STAMP(idx) do {} while (0)
constexpr int MEM_LDS_STRIDE = 1032;

__global__ __launch_bounds__(512, 2) void chain_kernel(ChainArgs a) {
  extern __shared__ __attribute__((aligned(16))) char lds[];
  const int b = blockIdx.x;
  const int tid = threadIdx.x, lane = tid & 63;
  const int w = __builtin_amdgcn_readfirstlane(tid >> 6);
  gu64* gMem = (gu64*)a.gMem;
  gu64* gQK = (gu64*)a.gQK;
  gu64* gO = (gu64*)a.gO;
  gu32* tmo = (gu32*)a.tmo;

  if (b < 192) {
    const int t = b / 64, cb = b % 64;
    const u16* W = t == 0 ? a.Wq : (t == 1 ? a.Wk : a.Wv);
    const int nt = w & 3, kh = w >> 2;
    const int col = cb * 64 + nt * 16 + (lane & 15);
    bf16x8 wf[16];
#pragma unroll
    for (int i = 0; i < 16; ++i) {
#pragma unroll
      for (int j = 0; j < 8; ++j) wf[i][j] = (short)W[(size_t)(kh * 512 + i * 32 + (lane >> 4) * 8 + j) * B_HD + col];
      asm volatile("" : "+v"(wf[i]));
    }
    u16* sMem = (u16*)lds;
    u16* sQ = (u16*)(lds + 16896);
    u16* sK = sQ + 8 * 136;
    u16* sVT = sK + 48 * 136;
    f32x4* sRedP = (f32x4*)(sVT + 64 * 56);
    if (t == 2) {
      for (int i = tid; i < 8 * 136; i += 512) sK[40 * 136 + i] = 0;
      for (int i = tid; i < 64 * 8; i += 512) sVT[(i >> 3) * 56 + 40 + (i & 7)] = 0;
    }
    const int h = cb >> 1, half = cb & 1;
    for (int s = 0; s < B_NSEG; ++s) {
      const unsigned epoch = (unsigned)s + 1u;
      u32x4 kxr0 = (u32x4){0, 0, 0, 0}, vxr = (u32x4){0, 0, 0, 0};
      if (t == 2) {
        {
          int r = tid >> 4, ch = tid & 15;
          kxr0 = *reinterpret_cast<const u32x4*>(a.qkvB + (size_t)(s * 32 + r) * QKVB_LD + B_HD + h * 128 + ch * 8);
        }
        if (tid < 256) {
          int r = tid >> 3, ch = tid & 7;
          vxr = *reinterpret_cast<const u32x4*>(a.qkvB + (size_t)(s * 32 + r) * QKVB_LD + 2 * B_HD + h * 128 +
                                                half * 64 + ch * 8);
        }
      }
      STAMP(0);
      if (s == 0) {
#pragma unroll
        for (int k = 0; k < 8; ++k) {
          int g = tid + 512 * k;
          int r = g >> 9, cp = g & 511;
          unsigned v = *reinterpret_cast<const unsigned*>(a.mem0 + r * 1024 + cp * 2);
          *reinterpret_cast<unsigned*>(sMem + r * MEM_LDS_STRIDE + cp * 2) = v;
        }
      } else {
        unsigned v[8];
        if (!sweep_granules<8>(gMem + tid, 512, (unsigned)s, v, tmo, lane)) return;
#pragma unroll
        for (int k = 0; k < 8; ++k) {
          int g = tid + 512 * k;
          int r = g >> 9, cp = g & 511;
          *reinterpret_cast<unsigned*>(sMem + r * MEM_LDS_STRIDE + cp * 2) = v[k];
        }
      }
      __syncthreads();
      STAMP(1);
      f32x4 acc = (f32x4){0.f, 0.f, 0.f, 0.f}, acc2 = (f32x4){0.f, 0.f, 0.f, 0.f};
      {
        bf16x8 af[16];
#pragma unroll
        for (int i = 0; i < 16; ++i)
          af[i] = *reinterpret_cast<const bf16x8*>(sMem + (lane & 7) * MEM_LDS_STRIDE + kh * 512 + i * 32 + (lane >> 4) * 8);
#pragma unroll
        for (int i = 0; i < 16; i += 2) {
          acc = __builtin_amdgcn_mfma_f32_16x16x32_bf16(wf[i], af[i], acc, 0, 0, 0);
          acc2 = __builtin_amdgcn_mfma_f32_16x16x32_bf16(wf[i + 1], af[i + 1], acc2, 0, 0, 0);
        }
        acc += acc2;
      }
      if (kh == 1) sRedP[nt * 64 + lane] = acc;
      __syncthreads();
      if (kh == 0) {
        acc += sRedP[nt * 64 + lane];
      {
        if ((lane & 15) < 8) {
          const int r = lane & 15;
#pragma unroll
          for (int jj = 0; jj < 2; ++jj) {
            const unsigned pk = (unsigned)f2bf(acc[2 * jj]) | ((unsigned)f2bf(acc[2 * jj + 1]) << 16);
            const int cp = nt * 8 + (lane >> 4) * 2 + jj;
            if (t < 2) store_granule(gQK + (size_t)t * 16384 + r * 2048 + cb * 32 + cp, epoch, pk);
            if (t >= 1)
              *reinterpret_cast<unsigned*>(a.kvm + ((size_t)(s * 2 + (t - 1)) * 8 + r) * B_HD + cb * 64 + cp * 2) = pk;
            if (t == 2) {
              sVT[(cp * 2) * 56 + r] = (u16)(pk & 0xffff);
              sVT[(cp * 2 + 1) * 56 + r] = (u16)(pk >> 16);
            }
          }
        }
      }
      }
      STAMP(2);
      if (t == 2) {
        {
          int r = tid >> 4, ch = tid & 15;
          *reinterpret_cast<u32x4*>(sK + (8 + r) * 136 + ch * 8) = kxr0;
        }
        if (tid < 256) {
          int r = tid >> 3, ch = tid & 7;
#pragma unroll
          for (int e = 0; e < 4; ++e) {
            sVT[(ch * 8 + 2 * e) * 56 + 8 + r] = (u16)(vxr[e] & 0xffff);
            sVT[(ch * 8 + 2 * e + 1) * 56 + 8 + r] = (u16)(vxr[e] >> 16);
          }
        }
        {
          unsigned v[2];
          int r = tid >> 6, cp = tid & 63;
          if (!sweep_granules<2>(gQK + r * 2048 + h * 64 + cp, 16384, epoch, v, tmo, lane)) return;
          *reinterpret_cast<unsigned*>(sQ + r * 136 + cp * 2) = v[0];
          *reinterpret_cast<unsigned*>(sK + r * 136 + cp * 2) = v[1];
        }
        __syncthreads();
        STAMP(3);
        if (w == 0) {
          bf16x8 qf[4], kf[3][4];
#pragma unroll
          for (int ks = 0; ks < 4; ++ks)
            qf[ks] = *reinterpret_cast<const bf16x8*>(sQ + (lane & 7) * 136 + ks * 32 + (lane >> 4) * 8);
#pragma unroll
          for (int tt = 0; tt < 3; ++tt)
#pragma unroll
            for (int ks = 0; ks < 4; ++ks)
              kf[tt][ks] = *reinterpret_cast<const bf16x8*>(sK + (tt * 16 + (lane & 15)) * 136 + ks * 32 + (lane >> 4) * 8);
          bf16x4 vlo[4], vmid[4], vhi[4];
#pragma unroll
          for (int n = 0; n < 4; ++n) {
            const u16* vp = sVT + (n * 16 + (lane & 15)) * 56 + (lane >> 4) * 4;
            vlo[n] = *reinterpret_cast<const bf16x4*>(vp);
            vmid[n] = *reinterpret_cast<const bf16x4*>(vp + 16);
            vhi[n] = *reinterpret_cast<const bf16x4*>(vp + 32);
          }
          f32x4 sc[3];
          float mx = -3e38f;
#pragma unroll
          for (int tt = 0; tt < 3; ++tt) {
            f32x4 sv = (f32x4){0.f, 0.f, 0.f, 0.f};
#pragma unroll
            for (int ks = 0; ks < 4; ++ks) sv = __builtin_amdgcn_mfma_f32_16x16x32_bf16(kf[tt][ks], qf[ks], sv, 0, 0, 0);
#pragma unroll
            for (int j = 0; j < 4; ++j) {
              const int key = tt * 16 + (lane >> 4) * 4 + j;
              float v = rbf(rbf(sv[j]) * 0.08838834764831845f);
              v = key < 40 ? v : -3e38f;
              sv[j] = v;
              mx = fmaxf(mx, v);
            }
            sc[tt] = sv;
          }
          mx = fmaxf(mx, __shfl_xor(mx, 16));
          mx = fmaxf(mx, __shfl_xor(mx, 32));
          float sum = 0.f;
#pragma unroll
          for (int tt = 0; tt < 3; ++tt)
#pragma unroll
            for (int j = 0; j < 4; ++j) {
              float e = __expf(sc[tt][j] - mx);
              sc[tt][j] = e;
              sum += e;
            }
          sum += __shfl_xor(sum, 16);
          sum += __shfl_xor(sum, 32);
          const float inv = 1.f / sum;
          bf16x8 p0, p1;
#pragma unroll
          for (int j = 0; j < 4; ++j) {
            p0[j] = (short)f2bf(sc[0][j] * inv);
            p0[4 + j] = (short)f2bf(sc[1][j] * inv);
            p1[j] = (short)f2bf(sc[2][j] * inv);
            p1[4 + j] = 0;
          }
#pragma unroll
          for (int n = 0; n < 4; ++n) {
            bf16x8 va = (bf16x8){vlo[n][0], vlo[n][1], vlo[n][2], vlo[n][3], vmid[n][0], vmid[n][1], vmid[n][2], vmid[n][3]};
            bf16x8 vb = (bf16x8){vhi[n][0], vhi[n][1], vhi[n][2], vhi[n][3], 0, 0, 0, 0};
            f32x4 oacc = __builtin_amdgcn_mfma_f32_16x16x32_bf16(va, p0, (f32x4){0.f, 0.f, 0.f, 0.f}, 0, 0, 0);
            oacc = __builtin_amdgcn_mfma_f32_16x16x32_bf16(vb, p1, oacc, 0, 0, 0);
            if ((lane & 15) < 8) {
#pragma unroll
              for (int jj = 0; jj < 2; ++jj)
                store_granule(gO + (lane & 15) * 2048 + cb * 32 + n * 8 + (lane >> 4) * 2 + jj, epoch,
                              (unsigned)f2bf(oacc[2 * jj]) | ((unsigned)f2bf(oacc[2 * jj + 1]) << 16));
            }
          }
          STAMP(4);
        }
      }
      __syncthreads();
    }
  } else {
    const int ob = b - 192;
    const int col = ob * 16 + (lane & 15);
    bf16x8 wf[16];
#pragma unroll
    for (int i = 0; i < 16; ++i) {
#pragma unroll
      for (int j = 0; j < 8; ++j)
        wf[i][j] = (short)a.Wo[(size_t)(w * 512 + i * 32 + (lane >> 4) * 8 + j) * D_MODEL + col];
      asm volatile("" : "+v"(wf[i]));
    }
    u16* sO = (u16*)lds + w * (8 * 520);
    float* sRed = (float*)(lds + 8 * 8 * 520 * 2);
    for (int s = 0; s < B_NSEG; ++s) {
      const unsigned epoch = (unsigned)s + 1u;
      STAMP(0);
      {
        unsigned v[32];
        unsigned spins = 0;
        for (;;) {
          bool ok = true;
#pragma unroll
          for (int k = 0; k < 32; ++k) {
            const unsigned long long x =
                __hip_atomic_load(gO + (k >> 2) * 2048 + w * 256 + (k & 3) * 64 + lane, RLX_AGENT);
            v[k] = (unsigned)x;
            ok &= (x >> 32) == epoch;
          }
          if (__all(ok)) break;
          if (spin_fail(spins, tmo, lane)) return;
        }
#pragma unroll
        for (int k = 0; k < 32; ++k)
          *reinterpret_cast<unsigned*>(sO + (k >> 2) * 520 + ((k & 3) * 64 + lane) * 2) = v[k];
      }
      __builtin_amdgcn_s_waitcnt(0xc07f);
      STAMP(1);
      f32x4 acc = (f32x4){0.f, 0.f, 0.f, 0.f}, acc2 = (f32x4){0.f, 0.f, 0.f, 0.f};
      {
        bf16x8 af[16];
#pragma unroll
        for (int i = 0; i < 16; ++i)
          af[i] = *reinterpret_cast<const bf16x8*>(sO + (lane & 7) * 520 + i * 32 + (lane >> 4) * 8);
#pragma unroll
        for (int i = 0; i < 16; i += 2) {
          acc = __builtin_amdgcn_mfma_f32_16x16x32_bf16(wf[i], af[i], acc, 0, 0, 0);
          acc2 = __builtin_amdgcn_mfma_f32_16x16x32_bf16(wf[i + 1], af[i + 1], acc2, 0, 0, 0);
        }
        acc += acc2;
      }
      if ((lane & 15) < 8) {
#pragma unroll
        for (int j = 0; j < 4; ++j) sRed[(w * 8 + (lane & 15)) * 16 + (lane >> 4) * 4 + j] = acc[j];
      }
      __syncthreads();
      if (tid < 64) {
        int r = tid >> 3, cp = tid & 7;
        float v0 = 0.f, v1 = 0.f;
#pragma unroll
        for (int q = 0; q < 8; ++q) {
          v0 += sRed[(q * 8 + r) * 16 + cp * 2];
          v1 += sRed[(q * 8 + r) * 16 + cp * 2 + 1];
        }
        unsigned pk = (unsigned)f2bf(v0) | ((unsigned)f2bf(v1) << 16);
        if (s + 1 < B_NSEG) store_granule(gMem + r * 512 + ob * 8 + cp, epoch, pk);
      }
      STAMP(2);
      __syncthreads();
    }
  }
}

extern "C" void kernel_launch(void* const* d_in, const int* in_sizes, int n_in, void* d_out, int out_size, void* d_ws,
                              size_t ws_size, hipStream_t stream) {
  const u16* A_x = (const u16*)d_in[0];
  const u16* A_wqkv = (const u16*)d_in[1];
  const u16* A_wo = (const u16*)d_in[2];
  const u16* B_x = (const u16*)d_in[3];
  const u16* B_mem0 = (const u16*)d_in[4];
  const u16* B_Wq = (const u16*)d_in[5];
  const u16* B_Wk = (const u16*)d_in[6];
  const u16* B_Wv = (const u16*)d_in[7];
  const u16* B_Wo = (const u16*)d_in[8];
  u16* out = (u16*)d_out;
  char* ws = (char*)d_ws;
  u16* qkvB = (u16*)ws;
  u16* qkvA = (u16*)(ws + (size_t)50331648);
  u16* oA = (u16*)(ws + (size_t)50331648 + 12582912);
  u16* kvm = (u16*)(ws + (size_t)67108864);
  u16* oB = (u16*)(ws + (size_t)75497472);
  char* gran = ws + (size_t)92274688;
  const size_t gran_bytes = 32768 + 262144 + 131072 + 256;
  (void)hipMemsetAsync(gran, 0, gran_bytes, stream);

  gemm128<4><<<(2048 / 128) * (4096 / 128), 256, 0, stream>>>(B_x, 1024, B_Wq, 4096, qkvB, QKVB_LD, 2048, 4096, 1024);
  gemm128<4><<<(2048 / 128) * (4096 / 128), 256, 0, stream>>>(B_x, 1024, B_Wk, 4096, qkvB + 4096, QKVB_LD, 2048, 4096, 1024);
  gemm128<4><<<(2048 / 128) * (4096 / 128), 256, 0, stream>>>(B_x, 1024, B_Wv, 4096, qkvB + 8192, QKVB_LD, 2048, 4096, 1024);
  ChainArgs ca;
  ca.mem0 = B_mem0;
  ca.Wq = B_Wq;
  ca.Wk = B_Wk;
  ca.Wv = B_Wv;
  ca.Wo = B_Wo;
  ca.qkvB = qkvB;
  ca.kvm = kvm;
  ca.gMem = (unsigned long long*)gran;
  ca.gQK = (unsigned long long*)(gran + 32768);
  ca.gO = (unsigned long long*)(gran + 32768 + 262144);
  ca.tmo = (unsigned*)(gran + 32768 + 262144 + 131072);
  (void)hipFuncSetAttribute((const void*)chain_kernel, hipFuncAttributeMaxDynamicSharedMemorySize, 96 * 1024);
  chain_kernel<<<256, 512, 96 * 1024, stream>>>(ca);
  attnBx<<<B_NSEG * 8, 256, 0, stream>>>(qkvB, kvm, oB);
  gemm128<2><<<(2048 / 64) * (1024 / 128), 256, 0, stream>>>(oB, 4096, B_Wo, 1024, out + 1024, OUT_LD, 2048, 1024, 4096);
  gemm128<4><<<(2048 / 128) * (3072 / 128), 256, 0, stream>>>(A_x, 1024, A_wqkv, 3072, qkvA, QKVA_LD, 2048, 3072, 1024);
  attnA<<<(2048 / 64) * A_H, 256, 0, stream>>>(qkvA, oA);
  gemm128<2><<<(2048 / 64) * (1024 / 128), 256, 0, stream>>>(oA, 1024, A_wo, 1024, out, OUT_LD, 2048, 1024, 1024);
}
```

```cpp
#include <hip/hip_runtime.h>
#include <hip/hip_bf16.h>
#include <stdint.h>

constexpr int S_LEN = 2048;
constexpr int D_MODEL = 1024;
constexpr int A_H = 32, A_DH = 32, A_W = 256;
constexpr int B_H = 32, B_DH = 128, B_M = 8, B_SEG = 32, B_NSEG = 64;
constexpr int B_HD = B_H * B_DH;
constexpr int QKVB_LD = 3 * B_HD;
constexpr int QKVA_LD = 3 * D_MODEL;
constexpr int OUT_LD = 2 * D_MODEL;

typedef unsigned short u16;
typedef __attribute__((ext_vector_type(8))) short bf16x8;
typedef __attribute__((ext_vector_type(4))) short bf16x4;
typedef __attribute__((ext_vector_type(4))) float f32x4;
typedef __attribute__((ext_vector_type(4))) unsigned u32x4;
#define GAS __attribute__((address_space(1)))
typedef GAS unsigned gu32;
typedef GAS unsigned long long gu64;
#define RLX_AGENT __ATOMIC_RELAXED, __HIP_MEMORY_SCOPE_AGENT

__device__ __forceinline__ float bf2f(u16 v) { return __uint_as_float(((unsigned)v) << 16); }
__device__ __forceinline__ u16 f2bf(float f) {
  __hip_bfloat16 b = __float2bfloat16(f);
  return *reinterpret_cast<u16*>(&b);
}
__device__ __forceinline__ float rbf(float f) { return bf2f(f2bf(f)); }

__device__ __forceinline__ unsigned boff(unsigned row, unsigned ch) {
  return 256u * row + 16u * (ch ^ (((row & 3) << 2) | ((row >> 2) & 3)));
}

template <int MT>
__global__ __launch_bounds__(256, 2) void gemm128(const u16* __restrict__ A, int lda,
                                                  const u16* __restrict__ B, int ldb,
                                                  u16* __restrict__ C, int ldc, int M, int N, int K) {
  constexpr int BM = MT * 32;
  __shared__ __attribute__((aligned(16))) char smem[2 * (8192 + 8192)];
  const int nbn = N / 128;
  const int bm = blockIdx.x / nbn, bn = blockIdx.x % nbn;
  const int tid = threadIdx.x, lane = tid & 63, wid = tid >> 6;
  const int wr = wid >> 1, wc = wid & 1;
  const int row0 = bm * BM, col0 = bn * 128;
  u32x4 ra[2], rb[2];
  auto gload = [&](int kt) {
#pragma unroll
    for (int i = 0; i < 2; ++i) {
      int c = tid + 256 * i;
      int r = c >> 2, ch = c & 3;
      if (r < BM) ra[i] = *reinterpret_cast<const u32x4*>(A + (size_t)(row0 + r) * lda + kt * 32 + ch * 8);
      int kr = c >> 4, chb = c & 15;
      rb[i] = *reinterpret_cast<const u32x4*>(B + (size_t)(kt * 32 + kr) * ldb + col0 + chb * 8);
    }
  };
  auto swrite = [&](int buf) {
    char* sa = smem + buf * 16384;
    char* sb = sa + 8192;
#pragma unroll
    for (int i = 0; i < 2; ++i) {
      int c = tid + 256 * i;
      int r = c >> 2, ch = c & 3;
      if (r < BM) *reinterpret_cast<u32x4*>(sa + r * 64 + ch * 16) = ra[i];
      int kr = c >> 4, chb = c & 15;
      *reinterpret_cast<u32x4*>(sb + boff(kr, chb)) = rb[i];
    }
  };
  f32x4 acc[MT][4];
#pragma unroll
  for (int m = 0; m < MT; ++m)
#pragma unroll
    for (int n = 0; n < 4; ++n) acc[m][n] = (f32x4){0.f, 0.f, 0.f, 0.f};
  const int nk = K / 32;
  gload(0);
  swrite(0);
  __syncthreads();
  for (int kt = 0; kt < nk; ++kt) {
    const int buf = kt & 1;
    if (kt + 1 < nk) gload(kt + 1);
    const char* sa = smem + buf * 16384;
    const char* sb = sa + 8192;
    bf16x8 af[MT], bfr[4];
#pragma unroll
    for (int m = 0; m < MT; ++m)
      af[m] = *reinterpret_cast<const bf16x8*>(sa + (wr * (MT * 16) + m * 16 + (lane & 15)) * 64 + (lane >> 4) * 16);
    {
      const unsigned g = lane >> 4, q = (lane & 15) >> 2, p = lane & 3;
      const unsigned base = (unsigned)(size_t)sb;
#pragma unroll
      for (int n = 0; n < 4; ++n) {
        const unsigned c = wc * 4 + n;
        unsigned a0 = base + boff(8 * g + 0 + q, 2 * c + (p >> 1)) + 8 * (p & 1);
        unsigned a1 = base + boff(8 * g + 4 + q, 2 * c + (p >> 1)) + 8 * (p & 1);
        bf16x4 lo, hi;
        asm volatile("ds_read_b64_tr_b16 %0, %2\n\tds_read_b64_tr_b16 %1, %3\n\ts_waitcnt lgkmcnt(0)"
                     : "=&v"(lo), "=&v"(hi)
                     : "v"(a0), "v"(a1)
                     : "memory");
        bfr[n] = (bf16x8){lo[0], lo[1], lo[2], lo[3], hi[0], hi[1], hi[2], hi[3]};
      }
    }
#pragma unroll
    for (int m = 0; m < MT; ++m)
#pragma unroll
      for (int n = 0; n < 4; ++n)
        acc[m][n] = __builtin_amdgcn_mfma_f32_16x16x32_bf16(af[m], bfr[n], acc[m][n], 0, 0, 0);
    if (kt + 1 < nk) swrite(buf ^ 1);
    __syncthreads();
  }
#pragma unroll
  for (int m = 0; m < MT; ++m)
#pragma unroll
    for (int n = 0; n < 4; ++n)
#pragma unroll
      for (int j = 0; j < 4; ++j)
        C[(size_t)(row0 + wr * (MT * 16) + m * 16 + (lane >> 4) * 4 + j) * ldc + col0 + wc * 64 + n * 16 + (lane & 15)] =
            f2bf(acc[m][n][j]);
}

__global__ __launch_bounds__(256, 2) void attnA(const u16* __restrict__ qkv, u16* __restrict__ o) {
  const int h = blockIdx.x % A_H, qb = blockIdx.x / A_H;
  const int tid = threadIdx.x, lane = tid & 63, wid = tid >> 6;
  const int q0 = qb * 64;
  const int kbase = q0 - 256;
  __shared__ __attribute__((aligned(16))) u16 sK[320 * 32];
  __shared__ __attribute__((aligned(16))) u16 sV[320 * 32];
  __shared__ __attribute__((aligned(16))) u16 sP[4][16 * 296];
  for (int c = tid; c < 320 * 4; c += 256) {
    int r = c >> 2, ch = c & 3;
    int key = kbase + r;
    u32x4 kv = (u32x4){0, 0, 0, 0}, vv = (u32x4){0, 0, 0, 0};
    if (key >= 0) {
      kv = *reinterpret_cast<const u32x4*>(qkv + (size_t)key * QKVA_LD + D_MODEL + h * 32 + ch * 8);
      vv = *reinterpret_cast<const u32x4*>(qkv + (size_t)key * QKVA_LD + 2 * D_MODEL + h * 32 + ch * 8);
    }
    *reinterpret_cast<u32x4*>(sK + r * 32 + ch * 8) = kv;
    *reinterpret_cast<u32x4*>(sV + r * 32 + ch * 8) = vv;
  }
  __syncthreads();
  const int qr0 = q0 + wid * 16;
  bf16x8 qf = *reinterpret_cast<const bf16x8*>(qkv + (size_t)(qr0 + (lane & 15)) * QKVA_LD + h * 32 + (lane >> 4) * 8);
  const int t0 = wid;
  f32x4 sc[17];
  const float scale = 0.17677669529663687f;
  float mx[4] = {-3e38f, -3e38f, -3e38f, -3e38f};
#pragma unroll
  for (int t = 0; t < 17; ++t) {
    bf16x8 kf = *reinterpret_cast<const bf16x8*>(sK + ((t0 + t) * 16 + (lane & 15)) * 32 + (lane >> 4) * 8);
    f32x4 s = __builtin_amdgcn_mfma_f32_16x16x32_bf16(qf, kf, (f32x4){0.f, 0.f, 0.f, 0.f}, 0, 0, 0);
    const int key = kbase + (t0 + t) * 16 + (lane & 15);
#pragma unroll
    for (int j = 0; j < 4; ++j) {
      const int row = qr0 + (lane >> 4) * 4 + j;
      float v = rbf(rbf(s[j]) * scale);
      const bool ok = (key <= row) && (row - key < A_W) && (key >= 0);
      v = ok ? v : -1e9f;
      s[j] = v;
      mx[j] = fmaxf(mx[j], v);
    }
    sc[t] = s;
  }
#pragma unroll
  for (int j = 0; j < 4; ++j) {
#pragma unroll
    for (int m = 1; m < 16; m <<= 1) mx[j] = fmaxf(mx[j], __shfl_xor(mx[j], m));
  }
  float sum[4] = {0.f, 0.f, 0.f, 0.f};
#pragma unroll
  for (int t = 0; t < 17; ++t)
#pragma unroll
    for (int j = 0; j < 4; ++j) {
      float e = __expf(sc[t][j] - mx[j]);
      sc[t][j] = e;
      sum[j] += e;
    }
#pragma unroll
  for (int j = 0; j < 4; ++j) {
#pragma unroll
    for (int m = 1; m < 16; m <<= 1) sum[j] += __shfl_xor(sum[j], m);
    sum[j] = 1.f / sum[j];
  }
  u16* P = sP[wid];
#pragma unroll
  for (int t = 0; t < 17; ++t)
#pragma unroll
    for (int j = 0; j < 4; ++j) P[((lane >> 4) * 4 + j) * 296 + t * 16 + (lane & 15)] = f2bf(sc[t][j] * sum[j]);
#pragma unroll
  for (int j = 0; j < 4; ++j) P[((lane >> 4) * 4 + j) * 296 + 272 + (lane & 15)] = 0;
  __syncthreads();
  f32x4 oacc[2] = {(f32x4){0.f, 0.f, 0.f, 0.f}, (f32x4){0.f, 0.f, 0.f, 0.f}};
  for (int ks = 0; ks < 9; ++ks) {
    bf16x8 pf = *reinterpret_cast<const bf16x8*>(P + (lane & 15) * 296 + ks * 32 + (lane >> 4) * 8);
#pragma unroll
    for (int n = 0; n < 2; ++n) {
      bf16x8 vf;
#pragma unroll
      for (int j = 0; j < 8; ++j) {
        int kl = t0 * 16 + ks * 32 + (lane >> 4) * 8 + j;
        kl = kl < 320 ? kl : 319;
        vf[j] = (short)sV[kl * 32 + n * 16 + (lane & 15)];
      }
      oacc[n] = __builtin_amdgcn_mfma_f32_16x16x32_bf16(pf, vf, oacc[n], 0, 0, 0);
    }
  }
#pragma unroll
  for (int n = 0; n < 2; ++n)
#pragma unroll
    for (int j = 0; j < 4; ++j)
      o[(size_t)(qr0 + (lane >> 4) * 4 + j) * D_MODEL + h * 32 + n * 16 + (lane & 15)] = f2bf(oacc[n][j]);
}

__global__ __launch_bounds__(256, 2) void attnBx(const u16* __restrict__ qkv, const u16* __restrict__ kvm,
                                                 u16* __restrict__ o) {
  const int seg = blockIdx.x / 8, hg = blockIdx.x % 8;
  const int tid = threadIdx.x, lane = tid & 63, wid = tid >> 6;
  const int h = hg * 4 + wid;
  __shared__ __attribute__((aligned(16))) u16 sK[4][48 * 136];
  __shared__ __attribute__((aligned(16))) u16 sV[4][64 * 136];
  __shared__ __attribute__((aligned(16))) u16 sP[4][32 * 72];
  u16* K = sK[wid];
  u16* V = sV[wid];
  u16* P = sP[wid];
  for (int c = lane; c < 64 * 16; c += 64) {
    int r = c >> 4, ch = c & 15;
    u32x4 kv = (u32x4){0, 0, 0, 0}, vv = (u32x4){0, 0, 0, 0};
    if (r < 8) {
      kv = *reinterpret_cast<const u32x4*>(kvm + ((size_t)(seg * 2 + 0) * 8 + r) * B_HD + h * 128 + ch * 8);
      vv = *reinterpret_cast<const u32x4*>(kvm + ((size_t)(seg * 2 + 1) * 8 + r) * B_HD + h * 128 + ch * 8);
    } else if (r < 40) {
      const u16* rowp = qkv + (size_t)(seg * 32 + r - 8) * QKVB_LD;
      kv = *reinterpret_cast<const u32x4*>(rowp + B_HD + h * 128 + ch * 8);
      vv = *reinterpret_cast<const u32x4*>(rowp + 2 * B_HD + h * 128 + ch * 8);
    }
    if (r < 48) *reinterpret_cast<u32x4*>(K + r * 136 + ch * 8) = kv;
    *reinterpret_cast<u32x4*>(V + r * 136 + ch * 8) = vv;
  }
  __syncthreads();
  const float scale = 0.08838834764831845f;
#pragma unroll
  for (int mt = 0; mt < 2; ++mt) {
    bf16x8 qf[4];
#pragma unroll
    for (int ks = 0; ks < 4; ++ks)
      qf[ks] = *reinterpret_cast<const bf16x8*>(qkv + (size_t)(seg * 32 + mt * 16 + (lane & 15)) * QKVB_LD + h * 128 +
                                                ks * 32 + (lane >> 4) * 8);
    f32x4 sc[3];
    float mx[4] = {-3e38f, -3e38f, -3e38f, -3e38f};
#pragma unroll
    for (int t = 0; t < 3; ++t) {
      f32x4 s = (f32x4){0.f, 0.f, 0.f, 0.f};
#pragma unroll
      for (int ks = 0; ks < 4; ++ks) {
        bf16x8 kf = *reinterpret_cast<const bf16x8*>(K + (t * 16 + (lane & 15)) * 136 + ks * 32 + (lane >> 4) * 8);
        s = __builtin_amdgcn_mfma_f32_16x16x32_bf16(qf[ks], kf, s, 0, 0, 0);
      }
      const int key = t * 16 + (lane & 15);
#pragma unroll
      for (int j = 0; j < 4; ++j) {
        const int rowp = 8 + mt * 16 + (lane >> 4) * 4 + j;
        float v = rbf(rbf(s[j]) * scale);
        const bool ok = (key <= rowp);
        v = ok ? v : -1e9f;
        s[j] = v;
        mx[j] = fmaxf(mx[j], v);
      }
      sc[t] = s;
    }
#pragma unroll
    for (int j = 0; j < 4; ++j) {
#pragma unroll
      for (int m = 1; m < 16; m <<= 1) mx[j] = fmaxf(mx[j], __shfl_xor(mx[j], m));
    }
    float sum[4] = {0.f, 0.f, 0.f, 0.f};
#pragma unroll
    for (int t = 0; t < 3; ++t)
#pragma unroll
      for (int j = 0; j < 4; ++j) {
        float e = __expf(sc[t][j] - mx[j]);
        sc[t][j] = e;
        sum[j] += e;
      }
#pragma unroll
    for (int j = 0; j < 4; ++j) {
#pragma unroll
      for (int m = 1; m < 16; m <<= 1) sum[j] += __shfl_xor(sum[j], m);
      sum[j] = 1.f / sum[j];
    }
#pragma unroll
    for (int t = 0; t < 3; ++t)
#pragma unroll
      for (int j = 0; j < 4; ++j)
        P[(mt * 16 + (lane >> 4) * 4 + j) * 72 + t * 16 + (lane & 15)] = f2bf(sc[t][j] * sum[j]);
#pragma unroll
    for (int j = 0; j < 4; ++j) P[(mt * 16 + (lane >> 4) * 4 + j) * 72 + 48 + (lane & 15)] = 0;
  }
  __syncthreads();
#pragma unroll
  for (int mt = 0; mt < 2; ++mt) {
    bf16x8 pf[2];
#pragma unroll
    for (int ks = 0; ks < 2; ++ks)
      pf[ks] = *reinterpret_cast<const bf16x8*>(P + (mt * 16 + (lane & 15)) * 72 + ks * 32 + (lane >> 4) * 8);
#pragma unroll
    for (int n = 0; n < 8; ++n) {
      f32x4 oacc = (f32x4){0.f, 0.f, 0.f, 0.f};
#pragma unroll
      for (int ks = 0; ks < 2; ++ks) {
        bf16x8 vf;
#pragma unroll
        for (int j = 0; j < 8; ++j) vf[j] = (short)V[(ks * 32 + (lane >> 4) * 8 + j) * 136 + n * 16 + (lane & 15)];
        oacc = __builtin_amdgcn_mfma_f32_16x16x32_bf16(pf[ks], vf, oacc, 0, 0, 0);
      }
#pragma unroll
      for (int j = 0; j < 4; ++j)
        o[(size_t)(seg * 32 + mt * 16 + (lane >> 4) * 4 + j) * B_HD + h * 128 + n * 16 + (lane & 15)] = f2bf(oacc[j]);
    }
  }
}

constexpr unsigned kSpinCap = 1u << 22;

__device__ __forceinline__ bool spin_fail(unsigned& spins, gu32* tmo, int lane) {
  __builtin_amdgcn_s_sleep(1);
  if (++spins > kSpinCap) {
    if (lane == 0) __hip_atomic_store(tmo, 1u, RLX_AGENT);
    return true;
  }
  return (spins & 255u) == 0u && __builtin_amdgcn_readfirstlane(__hip_atomic_load(tmo, RLX_AGENT));
}
__device__ __forceinline__ void store_granule(gu64* g, unsigned epoch, unsigned value) {
  __hip_atomic_store(g, ((unsigned long long)epoch << 32) | value, RLX_AGENT);
}
template <int N>
__device__ __forceinline__ bool sweep_granules(gu64* g, int S, unsigned epoch, unsigned (&v)[N], gu32* tmo, int lane) {
  for (unsigned spins = 0;;) {
    bool ok = true;
#pragma unroll
    for (int k = 0; k < N; ++k) {
      const unsigned long long x = __hip_atomic_load(g + k * S, RLX_AGENT);
      v[k] = (unsigned)x;
      ok &= (x >> 32) == epoch;
    }
    if (__all(ok)) return true;
    if (spin_fail(spins, tmo, lane)) return false;
  }
}

struct ChainArgs {
  const u16* mem0;
  const u16* Wq;
  const u16* Wk;
  const u16* Wv;
  const u16* Wo;
  const u16* qkvB;
  u16* kvm;
  unsigned long long* gMem;
  unsigned long long* gQK;
  unsigned long long* gO;
  unsigned* tmo;
};

#define STAMP(idx) do {} while (0)
constexpr int MEM_LDS_STRIDE = 1032;

__global__ __launch_bounds__(256, 1) void chain_kernel(ChainArgs a) {
  extern __shared__ __attribute__((aligned(16))) char lds[];
  const int b = blockIdx.x;
  const int tid = threadIdx.x, lane = tid & 63;
  const int w = __builtin_amdgcn_readfirstlane(tid >> 6);
  gu64* gMem = (gu64*)a.gMem;
  gu64* gQK = (gu64*)a.gQK;
  gu64* gO = (gu64*)a.gO;
  gu32* tmo = (gu32*)a.tmo;

  if (b < 192) {
    const int t = b / 64, cb = b % 64;
    const u16* W = t == 0 ? a.Wq : (t == 1 ? a.Wk : a.Wv);
    const int col = cb * 64 + w * 16 + (lane & 15);
    bf16x8 wf[32];
#pragma unroll
    for (int i = 0; i < 32; ++i) {
#pragma unroll
      for (int j = 0; j < 8; ++j) wf[i][j] = (short)W[(size_t)(i * 32 + (lane >> 4) * 8 + j) * B_HD + col];
      asm volatile("" : "+v"(wf[i]));
    }
    u16* sMem = (u16*)lds;
    u16* sQ = (u16*)(lds + 16896);
    u16* sK = sQ + 8 * 136;
    u16* sVT = sK + 48 * 136;
    if (t == 2) {
      for (int i = tid; i < 8 * 136; i += 256) sK[40 * 136 + i] = 0;
      for (int i = tid; i < 64 * 8; i += 256) sVT[(i >> 3) * 56 + 40 + (i & 7)] = 0;
    }
    const int h = cb >> 1, half = cb & 1;
    for (int s = 0; s < B_NSEG; ++s) {
      const unsigned epoch = (unsigned)s + 1u;
      u32x4 kxr0 = (u32x4){0, 0, 0, 0}, kxr1 = (u32x4){0, 0, 0, 0}, vxr = (u32x4){0, 0, 0, 0};
      if (t == 2) {
        {
          int r = tid >> 4, ch = tid & 15;
          kxr0 = *reinterpret_cast<const u32x4*>(a.qkvB + (size_t)(s * 32 + r) * QKVB_LD + B_HD + h * 128 + ch * 8);
          kxr1 = *reinterpret_cast<const u32x4*>(a.qkvB + (size_t)(s * 32 + 16 + r) * QKVB_LD + B_HD + h * 128 + ch * 8);
        }
        {
          int r = tid >> 3, ch = tid & 7;
          vxr = *reinterpret_cast<const u32x4*>(a.qkvB + (size_t)(s * 32 + r) * QKVB_LD + 2 * B_HD + h * 128 +
                                                half * 64 + ch * 8);
        }
      }
      STAMP(0);
      if (s == 0) {
#pragma unroll
        for (int k = 0; k < 16; ++k) {
          int g = tid + 256 * k;
          int r = g >> 9, cp = g & 511;
          unsigned v = *reinterpret_cast<const unsigned*>(a.mem0 + r * 1024 + cp * 2);
          *reinterpret_cast<unsigned*>(sMem + r * MEM_LDS_STRIDE + cp * 2) = v;
        }
      } else {
        unsigned v[16];
        {
          unsigned spins = 0;
          for (;;) {
            const unsigned long long x = __hip_atomic_load(gMem + 7 * 512 + lane * 8 + 7, RLX_AGENT);
            if (__all((x >> 32) == (unsigned)s)) break;
            if (spin_fail(spins, tmo, lane)) return;
          }
        }
        if (!sweep_granules<16>(gMem + tid, 256, (unsigned)s, v, tmo, lane)) return;
#pragma unroll
        for (int k = 0; k < 16; ++k) {
          int g = tid + 256 * k;
          int r = g >> 9, cp = g & 511;
          *reinterpret_cast<unsigned*>(sMem + r * MEM_LDS_STRIDE + cp * 2) = v[k];
        }
      }
      __syncthreads();
      STAMP(1);
      f32x4 acc = (f32x4){0.f, 0.f, 0.f, 0.f}, acc2 = (f32x4){0.f, 0.f, 0.f, 0.f};
      {
        bf16x8 af[32];
#pragma unroll
        for (int i = 0; i < 32; ++i)
          af[i] = *reinterpret_cast<const bf16x8*>(sMem + (lane & 7) * MEM_LDS_STRIDE + i * 32 + (lane >> 4) * 8);
#pragma unroll
        for (int i = 0; i < 32; i += 2) {
          acc = __builtin_amdgcn_mfma_f32_16x16x32_bf16(wf[i], af[i], acc, 0, 0, 0);
          acc2 = __builtin_amdgcn_mfma_f32_16x16x32_bf16(wf[i + 1], af[i + 1], acc2, 0, 0, 0);
        }
        acc += acc2;
      }
      {
        if ((lane & 15) < 8) {
          const int r = lane & 15;
#pragma unroll
          for (int jj = 0; jj < 2; ++jj) {
            const unsigned pk = (unsigned)f2bf(acc[2 * jj]) | ((unsigned)f2bf(acc[2 * jj + 1]) << 16);
            const int cp = w * 8 + (lane >> 4) * 2 + jj;
            if (t < 2) store_granule(gQK + (size_t)t * 16384 + r * 2048 + cb * 32 + cp, epoch, pk);
            if (t >= 1)
              *reinterpret_cast<unsigned*>(a.kvm + ((size_t)(s * 2 + (t - 1)) * 8 + r) * B_HD + cb * 64 + cp * 2) = pk;
            if (t == 2) {
              sVT[(cp * 2) * 56 + r] = (u16)(pk & 0xffff);
              sVT[(cp * 2 + 1) * 56 + r] = (u16)(pk >> 16);
            }
          }
        }
      }
      STAMP(2);
      if (t == 2) {
        {
          int r = tid >> 4, ch = tid & 15;
          *reinterpret_cast<u32x4*>(sK + (8 + r) * 136 + ch * 8) = kxr0;
          *reinterpret_cast<u32x4*>(sK + (24 + r) * 136 + ch * 8) = kxr1;
        }
        {
          int r = tid >> 3, ch = tid & 7;
#pragma unroll
          for (int e = 0; e < 4; ++e) {
            sVT[(ch * 8 + 2 * e) * 56 + 8 + r] = (u16)(vxr[e] & 0xffff);
            sVT[(ch * 8 + 2 * e + 1) * 56 + 8 + r] = (u16)(vxr[e] >> 16);
          }
        }
        {
          unsigned v[4];
          int r = tid >> 5, cp = tid & 31;
          {
            unsigned spins = 0;
            for (;;) {
              bool ok = true;
#pragma unroll
              for (int k = 0; k < 4; ++k) {
                const unsigned long long x = __hip_atomic_load(
                    gQK + (size_t)(k >> 1) * 16384 + r * 2048 + h * 64 + (k & 1) * 32 + cp, RLX_AGENT);
                v[k] = (unsigned)x;
                ok &= (x >> 32) == epoch;
              }
              if (__all(ok)) break;
              if (spin_fail(spins, tmo, lane)) return;
            }
          }
          *reinterpret_cast<unsigned*>(sQ + r * 136 + cp * 2) = v[0];
          *reinterpret_cast<unsigned*>(sQ + r * 136 + 64 + cp * 2) = v[1];
          *reinterpret_cast<unsigned*>(sK + r * 136 + cp * 2) = v[2];
          *reinterpret_cast<unsigned*>(sK + r * 136 + 64 + cp * 2) = v[3];
        }
        __syncthreads();
        STAMP(3);
        if (w == 0) {
          bf16x8 qf[4], kf[3][4];
#pragma unroll
          for (int ks = 0; ks < 4; ++ks)
            qf[ks] = *reinterpret_cast<const bf16x8*>(sQ + (lane & 7) * 136 + ks * 32 + (lane >> 4) * 8);
#pragma unroll
          for (int tt = 0; tt < 3; ++tt)
#pragma unroll
            for (int ks = 0; ks < 4; ++ks)
              kf[tt][ks] = *reinterpret_cast<const bf16x8*>(sK + (tt * 16 + (lane & 15)) * 136 + ks * 32 + (lane >> 4) * 8);
          bf16x4 vlo[4], vmid[4], vhi[4];
#pragma unroll
          for (int n = 0; n < 4; ++n) {
            const u16* vp = sVT + (n * 16 + (lane & 15)) * 56 + (lane >> 4) * 4;
            vlo[n] = *reinterpret_cast<const bf16x4*>(vp);
            vmid[n] = *reinterpret_cast<const bf16x4*>(vp + 16);
            vhi[n] = *reinterpret_cast<const bf16x4*>(vp + 32);
          }
          f32x4 sc[3];
          float mx = -3e38f;
#pragma unroll
          for (int tt = 0; tt < 3; ++tt) {
            f32x4 sv = (f32x4){0.f, 0.f, 0.f, 0.f};
#pragma unroll
            for (int ks = 0; ks < 4; ++ks) sv = __builtin_amdgcn_mfma_f32_16x16x32_bf16(kf[tt][ks], qf[ks], sv, 0, 0, 0);
#pragma unroll
            for (int j = 0; j < 4; ++j) {
              const int key = tt * 16 + (lane >> 4) * 4 + j;
              float v = rbf(rbf(sv[j]) * 0.08838834764831845f);
              v = key < 40 ? v : -3e38f;
              sv[j] = v;
              mx = fmaxf(mx, v);
            }
            sc[tt] = sv;
          }
          mx = fmaxf(mx, __shfl_xor(mx, 16));
          mx = fmaxf(mx, __shfl_xor(mx, 32));
          float sum = 0.f;
#pragma unroll
          for (int tt = 0; tt < 3; ++tt)
#pragma unroll
            for (int j = 0; j < 4; ++j) {
              float e = __expf(sc[tt][j] - mx);
              sc[tt][j] = e;
              sum += e;
            }
          sum += __shfl_xor(sum, 16);
          sum += __shfl_xor(sum, 32);
          const float inv = 1.f / sum;
          bf16x8 p0, p1;
#pragma unroll
          for (int j = 0; j < 4; ++j) {
            p0[j] = (short)f2bf(sc[0][j] * inv);
            p0[4 + j] = (short)f2bf(sc[1][j] * inv);
            p1[j] = (short)f2bf(sc[2][j] * inv);
            p1[4 + j] = 0;
          }
#pragma unroll
          for (int n = 0; n < 4; ++n) {
            bf16x8 va = (bf16x8){vlo[n][0], vlo[n][1], vlo[n][2], vlo[n][3], vmid[n][0], vmid[n][1], vmid[n][2], vmid[n][3]};
            bf16x8 vb = (bf16x8){vhi[n][0], vhi[n][1], vhi[n][2], vhi[n][3], 0, 0, 0, 0};
            f32x4 oacc = __builtin_amdgcn_mfma_f32_16x16x32_bf16(va, p0, (f32x4){0.f, 0.f, 0.f, 0.f}, 0, 0, 0);
            oacc = __builtin_amdgcn_mfma_f32_16x16x32_bf16(vb, p1, oacc, 0, 0, 0);
            if ((lane & 15) < 8) {
#pragma unroll
              for (int jj = 0; jj < 2; ++jj)
                store_granule(gO + (lane & 15) * 2048 + cb * 32 + n * 8 + (lane >> 4) * 2 + jj, epoch,
                              (unsigned)f2bf(oacc[2 * jj]) | ((unsigned)f2bf(oacc[2 * jj + 1]) << 16));
            }
          }
          STAMP(4);
        }
      }
      __syncthreads();
    }
  } else {
    const int ob = b - 192;
    const int col = ob * 16 + (lane & 15);
    bf16x8 wf[32];
#pragma unroll
    for (int i = 0; i < 32; ++i) {
#pragma unroll
      for (int j = 0; j < 8; ++j)
        wf[i][j] = (short)a.Wo[(size_t)(w * 1024 + i * 32 + (lane >> 4) * 8 + j) * D_MODEL + col];
      asm volatile("" : "+v"(wf[i]));
    }
    u16* sO = (u16*)lds + w * (8 * 1032);
    float* sRed = (float*)(lds + 4 * 8 * 1032 * 2);
    for (int s = 0; s < B_NSEG; ++s) {
      const unsigned epoch = (unsigned)s + 1u;
      STAMP(0);
      {
        unsigned spins = 0;
        for (;;) {
          const unsigned long long x = __hip_atomic_load(gO + 7 * 2048 + w * 512 + (lane & 15) * 32 + 31, RLX_AGENT);
          bool ok = (x >> 32) == epoch;
          if (__all(ok)) break;
          if (spin_fail(spins, tmo, lane)) return;
        }
      }
#pragma unroll
      for (int hb = 0; hb < 2; ++hb) {
        unsigned v[32];
        unsigned spins = 0;
        for (;;) {
          bool ok = true;
#pragma unroll
          for (int k = 0; k < 32; ++k) {
            const unsigned long long x =
                __hip_atomic_load(gO + (hb * 4 + (k >> 3)) * 2048 + w * 512 + (k & 7) * 64 + lane, RLX_AGENT);
            v[k] = (unsigned)x;
            ok &= (x >> 32) == epoch;
          }
          if (__all(ok)) break;
          if (spin_fail(spins, tmo, lane)) return;
        }
#pragma unroll
        for (int k = 0; k < 32; ++k)
          *reinterpret_cast<unsigned*>(sO + (hb * 4 + (k >> 3)) * 1032 + ((k & 7) * 64 + lane) * 2) = v[k];
      }
      __builtin_amdgcn_s_waitcnt(0xc07f);
      STAMP(1);
      f32x4 acc = (f32x4){0.f, 0.f, 0.f, 0.f}, acc2 = (f32x4){0.f, 0.f, 0.f, 0.f};
      {
        bf16x8 af[32];
#pragma unroll
        for (int i = 0; i < 32; ++i)
          af[i] = *reinterpret_cast<const bf16x8*>(sO + (lane & 7) * 1032 + i * 32 + (lane >> 4) * 8);
#pragma unroll
        for (int i = 0; i < 32; i += 2) {
          acc = __builtin_amdgcn_mfma_f32_16x16x32_bf16(wf[i], af[i], acc, 0, 0, 0);
          acc2 = __builtin_amdgcn_mfma_f32_16x16x32_bf16(wf[i + 1], af[i + 1], acc2, 0, 0, 0);
        }
        acc += acc2;
      }
      if ((lane & 15) < 8) {
#pragma unroll
        for (int j = 0; j < 4; ++j) sRed[(w * 8 + (lane & 15)) * 16 + (lane >> 4) * 4 + j] = acc[j];
      }
      __syncthreads();
      if (tid < 64) {
        int r = tid >> 3, cp = tid & 7;
        float v0 = 0.f, v1 = 0.f;
#pragma unroll
        for (int q = 0; q < 4; ++q) {
          v0 += sRed[(q * 8 + r) * 16 + cp * 2];
          v1 += sRed[(q * 8 + r) * 16 + cp * 2 + 1];
        }
        unsigned pk = (unsigned)f2bf(v0) | ((unsigned)f2bf(v1) << 16);
        if (s + 1 < B_NSEG) store_granule(gMem + r * 512 + ob * 8 + cp, epoch, pk);
      }
      STAMP(2);
      __syncthreads();
    }
  }
}

extern "C" void kernel_launch(void* const* d_in, const int* in_sizes, int n_in, void* d_out, int out_size, void* d_ws,
                              size_t ws_size, hipStream_t stream) {
  const u16* A_x = (const u16*)d_in[0];
  const u16* A_wqkv = (const u16*)d_in[1];
  const u16* A_wo = (const u16*)d_in[2];
  const u16* B_x = (const u16*)d_in[3];
  const u16* B_mem0 = (const u16*)d_in[4];
  const u16* B_Wq = (const u16*)d_in[5];
  const u16* B_Wk = (const u16*)d_in[6];
  const u16* B_Wv = (const u16*)d_in[7];
  const u16* B_Wo = (const u16*)d_in[8];
  u16* out = (u16*)d_out;
  char* ws = (char*)d_ws;
  u16* qkvB = (u16*)ws;
  u16* qkvA = (u16*)(ws + (size_t)50331648);
  u16* oA = (u16*)(ws + (size_t)50331648 + 12582912);
  u16* kvm = (u16*)(ws + (size_t)67108864);
  u16* oB = (u16*)(ws + (size_t)75497472);
  char* gran = ws + (size_t)92274688;
  const size_t gran_bytes = 32768 + 262144 + 131072 + 256;
  (void)hipMemsetAsync(gran, 0, gran_bytes, stream);

  gemm128<4><<<(2048 / 128) * (4096 / 128), 256, 0, stream>>>(B_x, 1024, B_Wq, 4096, qkvB, QKVB_LD, 2048, 4096, 1024);
  gemm128<4><<<(2048 / 128) * (4096 / 128), 256, 0, stream>>>(B_x, 1024, B_Wk, 4096, qkvB + 4096, QKVB_LD, 2048, 4096, 1024);
  gemm128<4><<<(2048 / 128) * (4096 / 128), 256, 0, stream>>>(B_x, 1024, B_Wv, 4096, qkvB + 8192, QKVB_LD, 2048, 4096, 1024);
  ChainArgs ca;
  ca.mem0 = B_mem0;
  ca.Wq = B_Wq;
  ca.Wk = B_Wk;
  ca.Wv = B_Wv;
  ca.Wo = B_Wo;
  ca.qkvB = qkvB;
  ca.kvm = kvm;
  ca.gMem = (unsigned long long*)gran;
  ca.gQK = (unsigned long long*)(gran + 32768);
  ca.gO = (unsigned long long*)(gran + 32768 + 262144);
  ca.tmo = (unsigned*)(gran + 32768 + 262144 + 131072);
  (void)hipFuncSetAttribute((const void*)chain_kernel, hipFuncAttributeMaxDynamicSharedMemorySize, 96 * 1024);
  chain_kernel<<<256, 256, 96 * 1024, stream>>>(ca);
  attnBx<<<B_NSEG * 8, 256, 0, stream>>>(qkvB, kvm, oB);
  gemm128<2><<<(2048 / 64) * (1024 / 128), 256, 0, stream>>>(oB, 4096, B_Wo, 1024, out + 1024, OUT_LD, 2048, 1024, 4096);
  gemm128<4><<<(2048 / 128) * (3072 / 128), 256, 0, stream>>>(A_x, 1024, A_wqkv, 3072, qkvA, QKVA_LD, 2048, 3072, 1024);
  attnA<<<(2048 / 64) * A_H, 256, 0, stream>>>(qkvA, oA);
  gemm128<2><<<(2048 / 64) * (1024 / 128), 256, 0, stream>>>(oA, 1024, A_wo, 1024, out, OUT_LD, 2048, 1024, 1024);
}
```

```cpp
#include <hip/hip_runtime.h>
#include <hip/hip_bf16.h>
#include <stdint.h>

constexpr int S_LEN = 2048;
constexpr int D_MODEL = 1024;
constexpr int A_H = 32, A_DH = 32, A_W = 256;
constexpr int B_H = 32, B_DH = 128, B_M = 8, B_SEG = 32, B_NSEG = 64;
constexpr int B_HD = B_H * B_DH;
constexpr int QKVB_LD = 3 * B_HD;
constexpr int QKVA_LD = 3 * D_MODEL;
constexpr int OUT_LD = 2 * D_MODEL;

typedef unsigned short u16;
typedef __attribute__((ext_vector_type(8))) short bf16x8;
typedef __attribute__((ext_vector_type(4))) short bf16x4;
typedef __attribute__((ext_vector_type(4))) float f32x4;
typedef __attribute__((ext_vector_type(4))) unsigned u32x4;
#define GAS __attribute__((address_space(1)))
typedef GAS unsigned gu32;
typedef GAS unsigned long long gu64;
#define RLX_AGENT __ATOMIC_RELAXED, __HIP_MEMORY_SCOPE_AGENT

__device__ __forceinline__ float bf2f(u16 v) { return __uint_as_float(((unsigned)v) << 16); }
__device__ __forceinline__ u16 f2bf(float f) {
  __hip_bfloat16 b = __float2bfloat16(f);
  return *reinterpret_cast<u16*>(&b);
}
__device__ __forceinline__ float rbf(float f) { return bf2f(f2bf(f)); }

__device__ __forceinline__ unsigned boff(unsigned row, unsigned ch) {
  return 256u * row + 16u * (ch ^ (((row & 3) << 2) | ((row >> 2) & 3)));
}

__global__ __launch_bounds__(256, 2) void gemm128(const u16* __restrict__ A, int lda,
                                                  const u16* __restrict__ B, int ldb,
                                                  u16* __restrict__ C, int ldc, int M, int N, int K) {
  __shared__ __attribute__((aligned(16))) char smem[2 * (8192 + 8192)];
  const int nbn = N / 128;
  const int bm = blockIdx.x / nbn, bn = blockIdx.x % nbn;
  const int tid = threadIdx.x, lane = tid & 63, wid = tid >> 6;
  const int wr = wid >> 1, wc = wid & 1;
  const int row0 = bm * 128, col0 = bn * 128;
  u32x4 ra[2], rb[2];
  auto gload = [&](int kt) {
#pragma unroll
    for (int i = 0; i < 2; ++i) {
      int c = tid + 256 * i;
      int r = c >> 2, ch = c & 3;
      ra[i] = *reinterpret_cast<const u32x4*>(A + (size_t)(row0 + r) * lda + kt * 32 + ch * 8);
      int kr = c >> 4, chb = c & 15;
      rb[i] = *reinterpret_cast<const u32x4*>(B + (size_t)(kt * 32 + kr) * ldb + col0 + chb * 8);
    }
  };
  auto swrite = [&](int buf) {
    char* sa = smem + buf * 16384;
    char* sb = sa + 8192;
#pragma unroll
    for (int i = 0; i < 2; ++i) {
      int c = tid + 256 * i;
      int r = c >> 2, ch = c & 3;
      *reinterpret_cast<u32x4*>(sa + r * 64 + ch * 16) = ra[i];
      int kr = c >> 4, chb = c & 15;
      *reinterpret_cast<u32x4*>(sb + boff(kr, chb)) = rb[i];
    }
  };
  f32x4 acc[4][4];
#pragma unroll
  for (int m = 0; m < 4; ++m)
#pragma unroll
    for (int n = 0; n < 4; ++n) acc[m][n] = (f32x4){0.f, 0.f, 0.f, 0.f};
  const int nk = K / 32;
  gload(0);
  swrite(0);
  __syncthreads();
  for (int kt = 0; kt < nk; ++kt) {
    const int buf = kt & 1;
    if (kt + 1 < nk) gload(kt + 1);
    const char* sa = smem + buf * 16384;
    const char* sb = sa + 8192;
    bf16x8 af[4], bfr[4];
#pragma unroll
    for (int m = 0; m < 4; ++m)
      af[m] = *reinterpret_cast<const bf16x8*>(sa + (wr * 64 + m * 16 + (lane & 15)) * 64 + (lane >> 4) * 16);
    {
      const unsigned g = lane >> 4, q = (lane & 15) >> 2, p = lane & 3;
      const unsigned base = (unsigned)(size_t)sb;
#pragma unroll
      for (int n = 0; n < 4; ++n) {
        const unsigned c = wc * 4 + n;
        unsigned a0 = base + boff(8 * g + 0 + q, 2 * c + (p >> 1)) + 8 * (p & 1);
        unsigned a1 = base + boff(8 * g + 4 + q, 2 * c + (p >> 1)) + 8 * (p & 1);
        bf16x4 lo, hi;
        asm volatile("ds_read_b64_tr_b16 %0, %2\n\tds_read_b64_tr_b16 %1, %3\n\ts_waitcnt lgkmcnt(0)"
                     : "=&v"(lo), "=&v"(hi)
                     : "v"(a0), "v"(a1)
                     : "memory");
        bfr[n] = (bf16x8){lo[0], lo[1], lo[2], lo[3], hi[0], hi[1], hi[2], hi[3]};
      }
    }
#pragma unroll
    for (int m = 0; m < 4; ++m)
#pragma unroll
      for (int n = 0; n < 4; ++n)
        acc[m][n] = __builtin_amdgcn_mfma_f32_16x16x32_bf16(af[m], bfr[n], acc[m][n], 0, 0, 0);
    if (kt + 1 < nk) swrite(buf ^ 1);
    __syncthreads();
  }
#pragma unroll
  for (int m = 0; m < 4; ++m)
#pragma unroll
    for (int n = 0; n < 4; ++n)
#pragma unroll
      for (int j = 0; j < 4; ++j)
        C[(size_t)(row0 + wr * 64 + m * 16 + (lane >> 4) * 4 + j) * ldc + col0 + wc * 64 + n * 16 + (lane & 15)] =
            f2bf(acc[m][n][j]);
}

__global__ __launch_bounds__(256, 2) void attnA(const u16* __restrict__ qkv, u16* __restrict__ o) {
  const int h = blockIdx.x % A_H, qb = blockIdx.x / A_H;
  const int tid = threadIdx.x, lane = tid & 63, wid = tid >> 6;
  const int q0 = qb * 64;
  const int kbase = q0 - 256;
  __shared__ __attribute__((aligned(16))) u16 sK[320 * 32];
  __shared__ __attribute__((aligned(16))) u16 sV[320 * 32];
  __shared__ __attribute__((aligned(16))) u16 sP[4][16 * 296];
  for (int c = tid; c < 320 * 4; c += 256) {
    int r = c >> 2, ch = c & 3;
    int key = kbase + r;
    u32x4 kv = (u32x4){0, 0, 0, 0}, vv = (u32x4){0, 0, 0, 0};
    if (key >= 0) {
      kv = *reinterpret_cast<const u32x4*>(qkv + (size_t)key * QKVA_LD + D_MODEL + h * 32 + ch * 8);
      vv = *reinterpret_cast<const u32x4*>(qkv + (size_t)key * QKVA_LD + 2 * D_MODEL + h * 32 + ch * 8);
    }
    *reinterpret_cast<u32x4*>(sK + r * 32 + ch * 8) = kv;
    *reinterpret_cast<u32x4*>(sV + r * 32 + ch * 8) = vv;
  }
  __syncthreads();
  const int qr0 = q0 + wid * 16;
  bf16x8 qf = *reinterpret_cast<const bf16x8*>(qkv + (size_t)(qr0 + (lane & 15)) * QKVA_LD + h * 32 + (lane >> 4) * 8);
  const int t0 = wid;
  f32x4 sc[17];
  const float scale = 0.17677669529663687f;
  float mx[4] = {-3e38f, -3e38f, -3e38f, -3e38f};
#pragma unroll
  for (int t = 0; t < 17; ++t) {
    bf16x8 kf = *reinterpret_cast<const bf16x8*>(sK + ((t0 + t) * 16 + (lane & 15)) * 32 + (lane >> 4) * 8);
    f32x4 s = __builtin_amdgcn_mfma_f32_16x16x32_bf16(qf, kf, (f32x4){0.f, 0.f, 0.f, 0.f}, 0, 0, 0);
    const int key = kbase + (t0 + t) * 16 + (lane & 15);
#pragma unroll
    for (int j = 0; j < 4; ++j) {
      const int row = qr0 + (lane >> 4) * 4 + j;
      float v = rbf(rbf(s[j]) * scale);
      const bool ok = (key <= row) && (row - key < A_W) && (key >= 0);
      v = ok ? v : -1e9f;
      s[j] = v;
      mx[j] = fmaxf(mx[j], v);
    }
    sc[t] = s;
  }
#pragma unroll
  for (int j = 0; j < 4; ++j) {
#pragma unroll
    for (int m = 1; m < 16; m <<= 1) mx[j] = fmaxf(mx[j], __shfl_xor(mx[j], m));
  }
  float sum[4] = {0.f, 0.f, 0.f, 0.f};
#pragma unroll
  for (int t = 0; t < 17; ++t)
#pragma unroll
    for (int j = 0; j < 4; ++j) {
      float e = __expf(sc[t][j] - mx[j]);
      sc[t][j] = e;
      sum[j] += e;
    }
#pragma unroll
  for (int j = 0; j < 4; ++j) {
#pragma unroll
    for (int m = 1; m < 16; m <<= 1) sum[j] += __shfl_xor(sum[j], m);
    sum[j] = 1.f / sum[j];
  }
  u16* P = sP[wid];
#pragma unroll
  for (int t = 0; t < 17; ++t)
#pragma unroll
    for (int j = 0; j < 4; ++j) P[((lane >> 4) * 4 + j) * 296 + t * 16 + (lane & 15)] = f2bf(sc[t][j] * sum[j]);
#pragma unroll
  for (int j = 0; j < 4; ++j) P[((lane >> 4) * 4 + j) * 296 + 272 + (lane & 15)] = 0;
  __syncthreads();
  f32x4 oacc[2] = {(f32x4){0.f, 0.f, 0.f, 0.f}, (f32x4){0.f, 0.f, 0.f, 0.f}};
  for (int ks = 0; ks < 9; ++ks) {
    bf16x8 pf = *reinterpret_cast<const bf16x8*>(P + (lane & 15) * 296 + ks * 32 + (lane >> 4) * 8);
#pragma unroll
    for (int n = 0; n < 2; ++n) {
      bf16x8 vf;
#pragma unroll
      for (int j = 0; j < 8; ++j) {
        int kl = t0 * 16 + ks * 32 + (lane >> 4) * 8 + j;
        kl = kl < 320 ? kl : 319;
        vf[j] = (short)sV[kl * 32 + n * 16 + (lane & 15)];
      }
      oacc[n] = __builtin_amdgcn_mfma_f32_16x16x32_bf16(pf, vf, oacc[n], 0, 0, 0);
    }
  }
#pragma unroll
  for (int n = 0; n < 2; ++n)
#pragma unroll
    for (int j = 0; j < 4; ++j)
      o[(size_t)(qr0 + (lane >> 4) * 4 + j) * D_MODEL + h * 32 + n * 16 + (lane & 15)] = f2bf(oacc[n][j]);
}

__global__ __launch_bounds__(256, 2) void attnBx(const u16* __restrict__ qkv, const u16* __restrict__ kvm,
                                                 u16* __restrict__ o) {
  const int seg = blockIdx.x / 8, hg = blockIdx.x % 8;
  const int tid = threadIdx.x, lane = tid & 63, wid = tid >> 6;
  const int h = hg * 4 + wid;
  __shared__ __attribute__((aligned(16))) u16 sK[4][48 * 136];
  __shared__ __attribute__((aligned(16))) u16 sV[4][64 * 136];
  __shared__ __attribute__((aligned(16))) u16 sP[4][32 * 72];
  u16* K = sK[wid];
  u16* V = sV[wid];
  u16* P = sP[wid];
  for (int c = lane; c < 64 * 16; c += 64) {
    int r = c >> 4, ch = c & 15;
    u32x4 kv = (u32x4){0, 0, 0, 0}, vv = (u32x4){0, 0, 0, 0};
    if (r < 8) {
      kv = *reinterpret_cast<const u32x4*>(kvm + ((size_t)(seg * 2 + 0) * 8 + r) * B_HD + h * 128 + ch * 8);
      vv = *reinterpret_cast<const u32x4*>(kvm + ((size_t)(seg * 2 + 1) * 8 + r) * B_HD + h * 128 + ch * 8);
    } else if (r < 40) {
      const u16* rowp = qkv + (size_t)(seg * 32 + r - 8) * QKVB_LD;
      kv = *reinterpret_cast<const u32x4*>(rowp + B_HD + h * 128 + ch * 8);
      vv = *reinterpret_cast<const u32x4*>(rowp + 2 * B_HD + h * 128 + ch * 8);
    }
    if (r < 48) *reinterpret_cast<u32x4*>(K + r * 136 + ch * 8) = kv;
    *reinterpret_cast<u32x4*>(V + r * 136 + ch * 8) = vv;
  }
  __syncthreads();
  const float scale = 0.08838834764831845f;
#pragma unroll
  for (int mt = 0; mt < 2; ++mt) {
    bf16x8 qf[4];
#pragma unroll
    for (int ks = 0; ks < 4; ++ks)
      qf[ks] = *reinterpret_cast<const bf16x8*>(qkv + (size_t)(seg * 32 + mt * 16 + (lane & 15)) * QKVB_LD + h * 128 +
                                                ks * 32 + (lane >> 4) * 8);
    f32x4 sc[3];
    float mx[4] = {-3e38f, -3e38f, -3e38f, -3e38f};
#pragma unroll
    for (int t = 0; t < 3; ++t) {
      f32x4 s = (f32x4){0.f, 0.f, 0.f, 0.f};
#pragma unroll
      for (int ks = 0; ks < 4; ++ks) {
        bf16x8 kf = *reinterpret_cast<const bf16x8*>(K + (t * 16 + (lane & 15)) * 136 + ks * 32 + (lane >> 4) * 8);
        s = __builtin_amdgcn_mfma_f32_16x16x32_bf16(qf[ks], kf, s, 0, 0, 0);
      }
      const int key = t * 16 + (lane & 15);
#pragma unroll
      for (int j = 0; j < 4; ++j) {
        const int rowp = 8 + mt * 16 + (lane >> 4) * 4 + j;
        float v = rbf(rbf(s[j]) * scale);
        const bool ok = (key <= rowp);
        v = ok ? v : -1e9f;
        s[j] = v;
        mx[j] = fmaxf(mx[j], v);
      }
      sc[t] = s;
    }
#pragma unroll
    for (int j = 0; j < 4; ++j) {
#pragma unroll
      for (int m = 1; m < 16; m <<= 1) mx[j] = fmaxf(mx[j], __shfl_xor(mx[j], m));
    }
    float sum[4] = {0.f, 0.f, 0.f, 0.f};
#pragma unroll
    for (int t = 0; t < 3; ++t)
#pragma unroll
      for (int j = 0; j < 4; ++j) {
        float e = __expf(sc[t][j] - mx[j]);
        sc[t][j] = e;
        sum[j] += e;
      }
#pragma unroll
    for (int j = 0; j < 4; ++j) {
#pragma unroll
      for (int m = 1; m < 16; m <<= 1) sum[j] += __shfl_xor(sum[j], m);
      sum[j] = 1.f / sum[j];
    }
#pragma unroll
    for (int t = 0; t < 3; ++t)
#pragma unroll
      for (int j = 0; j < 4; ++j)
        P[(mt * 16 + (lane >> 4) * 4 + j) * 72 + t * 16 + (lane & 15)] = f2bf(sc[t][j] * sum[j]);
#pragma unroll
    for (int j = 0; j < 4; ++j) P[(mt * 16 + (lane >> 4) * 4 + j) * 72 + 48 + (lane & 15)] = 0;
  }
  __syncthreads();
#pragma unroll
  for (int mt = 0; mt < 2; ++mt) {
    bf16x8 pf[2];
#pragma unroll
    for (int ks = 0; ks < 2; ++ks)
      pf[ks] = *reinterpret_cast<const bf16x8*>(P + (mt * 16 + (lane & 15)) * 72 + ks * 32 + (lane >> 4) * 8);
#pragma unroll
    for (int n = 0; n < 8; ++n) {
      f32x4 oacc = (f32x4){0.f, 0.f, 0.f, 0.f};
#pragma unroll
      for (int ks = 0; ks < 2; ++ks) {
        bf16x8 vf;
#pragma unroll
        for (int j = 0; j < 8; ++j) vf[j] = (short)V[(ks * 32 + (lane >> 4) * 8 + j) * 136 + n * 16 + (lane & 15)];
        oacc = __builtin_amdgcn_mfma_f32_16x16x32_bf16(pf[ks], vf, oacc, 0, 0, 0);
      }
#pragma unroll
      for (int j = 0; j < 4; ++j)
        o[(size_t)(seg * 32 + mt * 16 + (lane >> 4) * 4 + j) * B_HD + h * 128 + n * 16 + (lane & 15)] = f2bf(oacc[j]);
    }
  }
}

constexpr unsigned kSpinCap = 1u << 22;

__device__ __forceinline__ bool spin_fail(unsigned& spins, gu32* tmo, int lane) {
  __builtin_amdgcn_s_sleep(1);
  if (++spins > kSpinCap) {
    if (lane == 0) __hip_atomic_store(tmo, 1u, RLX_AGENT);
    return true;
  }
  return (spins & 255u) == 0u && __builtin_amdgcn_readfirstlane(__hip_atomic_load(tmo, RLX_AGENT));
}
__device__ __forceinline__ void store_granule(gu64* g, unsigned epoch, unsigned value) {
  __hip_atomic_store(g, ((unsigned long long)epoch << 32) | value, RLX_AGENT);
}
template <int N>
__device__ __forceinline__ bool sweep_granules(gu64* g, int S, unsigned epoch, unsigned (&v)[N], gu32* tmo, int lane) {
  for (unsigned spins = 0;;) {
    bool ok = true;
#pragma unroll
    for (int k = 0; k < N; ++k) {
      const unsigned long long x = __hip_atomic_load(g + k * S, RLX_AGENT);
      v[k] = (unsigned)x;
      ok &= (x >> 32) == epoch;
    }
    if (__all(ok)) return true;
    if (spin_fail(spins, tmo, lane)) return false;
  }
}

__device__ __forceinline__ u32x4 load16_sc1(__amdgpu_buffer_rsrc_t rs, unsigned byte_off) {
  return __builtin_amdgcn_raw_buffer_load_b128(rs, byte_off, 0, 16);
}
struct ChainArgs {
  const u16* mem0;
  const u16* Wq;
  const u16* Wk;
  const u16* Wv;
  const u16* Wo;
  const u16* qkvB;
  u16* kvm;
  unsigned long long* gMem;
  unsigned long long* gQK;
  unsigned long long* gO;
  unsigned* tmo;
};

#define STAMP(idx) do {} while (0)
constexpr int MEM_LDS_STRIDE = 1032;

__global__ __launch_bounds__(256, 1) void chain_kernel(ChainArgs a) {
  extern __shared__ __attribute__((aligned(16))) char lds[];
  const int b = blockIdx.x;
  const int tid = threadIdx.x, lane = tid & 63;
  const int w = __builtin_amdgcn_readfirstlane(tid >> 6);
  gu64* gMem = (gu64*)a.gMem;
  gu64* gQK = (gu64*)a.gQK;
  gu64* gO = (gu64*)a.gO;
  gu32* tmo = (gu32*)a.tmo;
  const __amdgpu_buffer_rsrc_t rsMem = __builtin_amdgcn_make_buffer_rsrc((void*)a.gMem, 0, 32768, 0x00020000);
  const __amdgpu_buffer_rsrc_t rsO = __builtin_amdgcn_make_buffer_rsrc((void*)a.gO, 0, 131072, 0x00020000);

  if (b < 192) {
    const int t = b / 64, cb = b % 64;
    const u16* W = t == 0 ? a.Wq : (t == 1 ? a.Wk : a.Wv);
    const int col = cb * 64 + w * 16 + (lane & 15);
    bf16x8 wf[32];
#pragma unroll
    for (int i = 0; i < 32; ++i) {
#pragma unroll
      for (int j = 0; j < 8; ++j) wf[i][j] = (short)W[(size_t)(i * 32 + (lane >> 4) * 8 + j) * B_HD + col];
      asm volatile("" : "+v"(wf[i]));
    }
    u16* sMem = (u16*)lds;
    u16* sQ = (u16*)(lds + 16896);
    u16* sK = sQ + 8 * 136;
    u16* sVT = sK + 48 * 136;
    if (t == 2) {
      for (int i = tid; i < 8 * 136; i += 256) sK[40 * 136 + i] = 0;
      for (int i = tid; i < 64 * 8; i += 256) sVT[(i >> 3) * 56 + 40 + (i & 7)] = 0;
    }
    const int h = cb >> 1, half = cb & 1;
    for (int s = 0; s < B_NSEG; ++s) {
      const unsigned epoch = (unsigned)s + 1u;
      u32x4 kxr0 = (u32x4){0, 0, 0, 0}, kxr1 = (u32x4){0, 0, 0, 0}, vxr = (u32x4){0, 0, 0, 0};
      if (t == 2) {
        {
          int r = tid >> 4, ch = tid & 15;
          kxr0 = *reinterpret_cast<const u32x4*>(a.qkvB + (size_t)(s * 32 + r) * QKVB_LD + B_HD + h * 128 + ch * 8);
          kxr1 = *reinterpret_cast<const u32x4*>(a.qkvB + (size_t)(s * 32 + 16 + r) * QKVB_LD + B_HD + h * 128 + ch * 8);
        }
        {
          int r = tid >> 3, ch = tid & 7;
          vxr = *reinterpret_cast<const u32x4*>(a.qkvB + (size_t)(s * 32 + r) * QKVB_LD + 2 * B_HD + h * 128 +
                                                half * 64 + ch * 8);
        }
      }
      STAMP(0);
      if (s == 0) {
#pragma unroll
        for (int k = 0; k < 16; ++k) {
          int g = tid + 256 * k;
          int r = g >> 9, cp = g & 511;
          unsigned v = *reinterpret_cast<const unsigned*>(a.mem0 + r * 1024 + cp * 2);
          *reinterpret_cast<unsigned*>(sMem + r * MEM_LDS_STRIDE + cp * 2) = v;
        }
      } else {
        {
          unsigned spins = 0;
          for (;;) {
            const unsigned long long x = __hip_atomic_load(gMem + 7 * 512 + lane * 8 + 7, RLX_AGENT);
            if (__all((x >> 32) == (unsigned)s)) break;
            if (spin_fail(spins, tmo, lane)) return;
          }
        }
        {
          u32x4 x[8];
          unsigned spins = 0;
          for (;;) {
#pragma unroll
            for (int k = 0; k < 8; ++k) x[k] = load16_sc1(rsMem, 16u * (tid + 256 * k));
            bool ok = true;
#pragma unroll
            for (int k = 0; k < 8; ++k) ok &= (x[k][1] == (unsigned)s) & (x[k][3] == (unsigned)s);
            if (__all(ok)) break;
            if (spin_fail(spins, tmo, lane)) return;
          }
#pragma unroll
          for (int k = 0; k < 8; ++k) {
            int g = 2 * (tid + 256 * k);
            int r = g >> 9, cp = g & 511;
            *reinterpret_cast<unsigned*>(sMem + r * MEM_LDS_STRIDE + cp * 2) = x[k][0];
            *reinterpret_cast<unsigned*>(sMem + r * MEM_LDS_STRIDE + cp * 2 + 2) = x[k][2];
          }
        }
      }
      __syncthreads();
      STAMP(1);
      f32x4 acc = (f32x4){0.f, 0.f, 0.f, 0.f}, acc2 = (f32x4){0.f, 0.f, 0.f, 0.f};
      {
        bf16x8 af[32];
#pragma unroll
        for (int i = 0; i < 32; ++i)
          af[i] = *reinterpret_cast<const bf16x8*>(sMem + (lane & 7) * MEM_LDS_STRIDE + i * 32 + (lane >> 4) * 8);
#pragma unroll
        for (int i = 0; i < 32; i += 2) {
          acc = __builtin_amdgcn_mfma_f32_16x16x32_bf16(wf[i], af[i], acc, 0, 0, 0);
          acc2 = __builtin_amdgcn_mfma_f32_16x16x32_bf16(wf[i + 1], af[i + 1], acc2, 0, 0, 0);
        }
        acc += acc2;
      }
      {
        if ((lane & 15) < 8) {
          const int r = lane & 15;
#pragma unroll
          for (int jj = 0; jj < 2; ++jj) {
            const unsigned pk = (unsigned)f2bf(acc[2 * jj]) | ((unsigned)f2bf(acc[2 * jj + 1]) << 16);
            const int cp = w * 8 + (lane >> 4) * 2 + jj;
            if (t < 2) store_granule(gQK + (size_t)t * 16384 + r * 2048 + cb * 32 + cp, epoch, pk);
            if (t >= 1)
              *reinterpret_cast<unsigned*>(a.kvm + ((size_t)(s * 2 + (t - 1)) * 8 + r) * B_HD + cb * 64 + cp * 2) = pk;
            if (t == 2) {
              sVT[(cp * 2) * 56 + r] = (u16)(pk & 0xffff);
              sVT[(cp * 2 + 1) * 56 + r] = (u16)(pk >> 16);
            }
          }
        }
      }
      STAMP(2);
      if (t == 2) {
        {
          int r = tid >> 4, ch = tid & 15;
          *reinterpret_cast<u32x4*>(sK + (8 + r) * 136 + ch * 8) = kxr0;
          *reinterpret_cast<u32x4*>(sK + (24 + r) * 136 + ch * 8) = kxr1;
        }
        {
          int r = tid >> 3, ch = tid & 7;
#pragma unroll
          for (int e = 0; e < 4; ++e) {
            sVT[(ch * 8 + 2 * e) * 56 + 8 + r] = (u16)(vxr[e] & 0xffff);
            sVT[(ch * 8 + 2 * e + 1) * 56 + 8 + r] = (u16)(vxr[e] >> 16);
          }
        }
        {
          unsigned v[4];
          int r = tid >> 5, cp = tid & 31;
          {
            unsigned spins = 0;
            for (;;) {
              bool ok = true;
#pragma unroll
              for (int k = 0; k < 4; ++k) {
                const unsigned long long x = __hip_atomic_load(
                    gQK + (size_t)(k >> 1) * 16384 + r * 2048 + h * 64 + (k & 1) * 32 + cp, RLX_AGENT);
                v[k] = (unsigned)x;
                ok &= (x >> 32) == epoch;
              }
              if (__all(ok)) break;
              if (spin_fail(spins, tmo, lane)) return;
            }
          }
          *reinterpret_cast<unsigned*>(sQ + r * 136 + cp * 2) = v[0];
          *reinterpret_cast<unsigned*>(sQ + r * 136 + 64 + cp * 2) = v[1];
          *reinterpret_cast<unsigned*>(sK + r * 136 + cp * 2) = v[2];
          *reinterpret_cast<unsigned*>(sK + r * 136 + 64 + cp * 2) = v[3];
        }
        __syncthreads();
        STAMP(3);
        if (w == 0) {
          bf16x8 qf[4], kf[3][4];
#pragma unroll
          for (int ks = 0; ks < 4; ++ks)
            qf[ks] = *reinterpret_cast<const bf16x8*>(sQ + (lane & 7) * 136 + ks * 32 + (lane >> 4) * 8);
#pragma unroll
          for (int tt = 0; tt < 3; ++tt)
#pragma unroll
            for (int ks = 0; ks < 4; ++ks)
              kf[tt][ks] = *reinterpret_cast<const bf16x8*>(sK + (tt * 16 + (lane & 15)) * 136 + ks * 32 + (lane >> 4) * 8);
          bf16x4 vlo[4], vmid[4], vhi[4];
#pragma unroll
          for (int n = 0; n < 4; ++n) {
            const u16* vp = sVT + (n * 16 + (lane & 15)) * 56 + (lane >> 4) * 4;
            vlo[n] = *reinterpret_cast<const bf16x4*>(vp);
            vmid[n] = *reinterpret_cast<const bf16x4*>(vp + 16);
            vhi[n] = *reinterpret_cast<const bf16x4*>(vp + 32);
          }
          f32x4 sc[3];
          float mx = -3e38f;
#pragma unroll
          for (int tt = 0; tt < 3; ++tt) {
            f32x4 sv = (f32x4){0.f, 0.f, 0.f, 0.f};
#pragma unroll
            for (int ks = 0; ks < 4; ++ks) sv = __builtin_amdgcn_mfma_f32_16x16x32_bf16(kf[tt][ks], qf[ks], sv, 0, 0, 0);
#pragma unroll
            for (int j = 0; j < 4; ++j) {
              const int key = tt * 16 + (lane >> 4) * 4 + j;
              float v = rbf(rbf(sv[j]) * 0.08838834764831845f);
              v = key < 40 ? v : -3e38f;
              sv[j] = v;
              mx = fmaxf(mx, v);
            }
            sc[tt] = sv;
          }
          mx = fmaxf(mx, __shfl_xor(mx, 16));
          mx = fmaxf(mx, __shfl_xor(mx, 32));
          float sum = 0.f;
#pragma unroll
          for (int tt = 0; tt < 3; ++tt)
#pragma unroll
            for (int j = 0; j < 4; ++j) {
              float e = __expf(sc[tt][j] - mx);
              sc[tt][j] = e;
              sum += e;
            }
          sum += __shfl_xor(sum, 16);
          sum += __shfl_xor(sum, 32);
          const float inv = 1.f / sum;
          bf16x8 p0, p1;
#pragma unroll
          for (int j = 0; j < 4; ++j) {
            p0[j] = (short)f2bf(sc[0][j] * inv);
            p0[4 + j] = (short)f2bf(sc[1][j] * inv);
            p1[j] = (short)f2bf(sc[2][j] * inv);
            p1[4 + j] = 0;
          }
#pragma unroll
          for (int n = 0; n < 4; ++n) {
            bf16x8 va = (bf16x8){vlo[n][0], vlo[n][1], vlo[n][2], vlo[n][3], vmid[n][0], vmid[n][1], vmid[n][2], vmid[n][3]};
            bf16x8 vb = (bf16x8){vhi[n][0], vhi[n][1], vhi[n][2], vhi[n][3], 0, 0, 0, 0};
            f32x4 oacc = __builtin_amdgcn_mfma_f32_16x16x32_bf16(va, p0, (f32x4){0.f, 0.f, 0.f, 0.f}, 0, 0, 0);
            oacc = __builtin_amdgcn_mfma_f32_16x16x32_bf16(vb, p1, oacc, 0, 0, 0);
            if ((lane & 15) < 8) {
#pragma unroll
              for (int jj = 0; jj < 2; ++jj)
                store_granule(gO + (lane & 15) * 2048 + cb * 32 + n * 8 + (lane >> 4) * 2 + jj, epoch,
                              (unsigned)f2bf(oacc[2 * jj]) | ((unsigned)f2bf(oacc[2 * jj + 1]) << 16));
            }
          }
          STAMP(4);
        }
      }
      __syncthreads();
    }
  } else {
    const int ob = b - 192;
    const int col = ob * 16 + (lane & 15);
    bf16x8 wf[32];
#pragma unroll
    for (int i = 0; i < 32; ++i) {
#pragma unroll
      for (int j = 0; j < 8; ++j)
        wf[i][j] = (short)a.Wo[(size_t)(w * 1024 + i * 32 + (lane >> 4) * 8 + j) * D_MODEL + col];
      asm volatile("" : "+v"(wf[i]));
    }
    u16* sO = (u16*)lds + w * (8 * 1032);
    float* sRed = (float*)(lds + 4 * 8 * 1032 * 2);
    for (int s = 0; s < B_NSEG; ++s) {
      const unsigned epoch = (unsigned)s + 1u;
      STAMP(0);
      {
        unsigned spins = 0;
        for (;;) {
          const unsigned long long x = __hip_atomic_load(gO + 7 * 2048 + w * 512 + (lane & 15) * 32 + 31, RLX_AGENT);
          bool ok = (x >> 32) == epoch;
          if (__all(ok)) break;
          if (spin_fail(spins, tmo, lane)) return;
        }
      }
      {
        u32x4 x[32];
        unsigned spins = 0;
        for (;;) {
#pragma unroll
          for (int k = 0; k < 32; ++k) x[k] = load16_sc1(rsO, 8u * ((k >> 2) * 2048 + w * 512 + ((k & 3) * 64 + lane) * 2));
          bool ok = true;
#pragma unroll
          for (int k = 0; k < 32; ++k) ok &= (x[k][1] == epoch) & (x[k][3] == epoch);
          if (__all(ok)) break;
          if (spin_fail(spins, tmo, lane)) return;
        }
#pragma unroll
        for (int k = 0; k < 32; ++k) {
          u16* d = sO + (k >> 2) * 1032 + ((k & 3) * 64 + lane) * 4;
          *reinterpret_cast<unsigned*>(d) = x[k][0];
          *reinterpret_cast<unsigned*>(d + 2) = x[k][2];
        }
      }
      __builtin_amdgcn_s_waitcnt(0xc07f);
      STAMP(1);
      f32x4 acc = (f32x4){0.f, 0.f, 0.f, 0.f}, acc2 = (f32x4){0.f, 0.f, 0.f, 0.f};
      {
        bf16x8 af[32];
#pragma unroll
        for (int i = 0; i < 32; ++i)
          af[i] = *reinterpret_cast<const bf16x8*>(sO + (lane & 7) * 1032 + i * 32 + (lane >> 4) * 8);
#pragma unroll
        for (int i = 0; i < 32; i += 2) {
          acc = __builtin_amdgcn_mfma_f32_16x16x32_bf16(wf[i], af[i], acc, 0, 0, 0);
          acc2 = __builtin_amdgcn_mfma_f32_16x16x32_bf16(wf[i + 1], af[i + 1], acc2, 0, 0, 0);
        }
        acc += acc2;
      }
      if ((lane & 15) < 8) {
#pragma unroll
        for (int j = 0; j < 4; ++j) sRed[(w * 8 + (lane & 15)) * 16 + (lane >> 4) * 4 + j] = acc[j];
      }
      __syncthreads();
      if (tid < 64) {
        int r = tid >> 3, cp = tid & 7;
        float v0 = 0.f, v1 = 0.f;
#pragma unroll
        for (int q = 0; q < 4; ++q) {
          v0 += sRed[(q * 8 + r) * 16 + cp * 2];
          v1 += sRed[(q * 8 + r) * 16 + cp * 2 + 1];
        }
        unsigned pk = (unsigned)f2bf(v0) | ((unsigned)f2bf(v1) << 16);
        if (s + 1 < B_NSEG) store_granule(gMem + r * 512 + ob * 8 + cp, epoch, pk);
      }
      STAMP(2);
      __syncthreads();
    }
  }
}

extern "C" void kernel_launch(void* const* d_in, const int* in_sizes, int n_in, void* d_out, int out_size, void* d_ws,
                              size_t ws_size, hipStream_t stream) {
  const u16* A_x = (const u16*)d_in[0];
  const u16* A_wqkv = (const u16*)d_in[1];
  const u16* A_wo = (const u16*)d_in[2];
  const u16* B_x = (const u16*)d_in[3];
  const u16* B_mem0 = (const u16*)d_in[4];
  const u16* B_Wq = (const u16*)d_in[5];
  const u16* B_Wk = (const u16*)d_in[6];
  const u16* B_Wv = (const u16*)d_in[7];
  const u16* B_Wo = (const u16*)d_in[8];
  u16* out = (u16*)d_out;
  char* ws = (char*)d_ws;
  u16* qkvB = (u16*)ws;
  u16* qkvA = (u16*)(ws + (size_t)50331648);
  u16* oA = (u16*)(ws + (size_t)50331648 + 12582912);
  u16* kvm = (u16*)(ws + (size_t)67108864);
  u16* oB = (u16*)(ws + (size_t)75497472);
  char* gran = ws + (size_t)92274688;
  const size_t gran_bytes = 32768 + 262144 + 131072 + 256;
  (void)hipMemsetAsync(gran, 0, gran_bytes, stream);

  gemm128<<<(2048 / 128) * (4096 / 128), 256, 0, stream>>>(B_x, 1024, B_Wq, 4096, qkvB, QKVB_LD, 2048, 4096, 1024);
  gemm128<<<(2048 / 128) * (4096 / 128), 256, 0, stream>>>(B_x, 1024, B_Wk, 4096, qkvB + 4096, QKVB_LD, 2048, 4096, 1024);
  gemm128<<<(2048 / 128) * (4096 / 128), 256, 0, stream>>>(B_x, 1024, B_Wv, 4096, qkvB + 8192, QKVB_LD, 2048, 4096, 1024);
  ChainArgs ca;
  ca.mem0 = B_mem0;
  ca.Wq = B_Wq;
  ca.Wk = B_Wk;
  ca.Wv = B_Wv;
  ca.Wo = B_Wo;
  ca.qkvB = qkvB;
  ca.kvm = kvm;
  ca.gMem = (unsigned long long*)gran;
  ca.gQK = (unsigned long long*)(gran + 32768);
  ca.gO = (unsigned long long*)(gran + 32768 + 262144);
  ca.tmo = (unsigned*)(gran + 32768 + 262144 + 131072);
  (void)hipFuncSetAttribute((const void*)chain_kernel, hipFuncAttributeMaxDynamicSharedMemorySize, 96 * 1024);
  chain_kernel<<<256, 256, 96 * 1024, stream>>>(ca);
  attnBx<<<B_NSEG * 8, 256, 0, stream>>>(qkvB, kvm, oB);
  gemm128<<<(2048 / 128) * (1024 / 128), 256, 0, stream>>>(oB, 4096, B_Wo, 1024, out + 1024, OUT_LD, 2048, 1024, 4096);
  gemm128<<<(2048 / 128) * (3072 / 128), 256, 0, stream>>>(A_x, 1024, A_wqkv, 3072, qkvA, QKVA_LD, 2048, 3072, 1024);
  attnA<<<(2048 / 64) * A_H, 256, 0, stream>>>(qkvA, oA);
  gemm128<<<(2048 / 128) * (1024 / 128), 256, 0, stream>>>(oA, 1024, A_wo, 1024, out, OUT_LD, 2048, 1024, 1024);
}
```

```cpp
#include <hip/hip_runtime.h>
#include <hip/hip_bf16.h>
#include <stdint.h>

constexpr int S_LEN = 2048;
constexpr int D_MODEL = 1024;
constexpr int A_H = 32, A_DH = 32, A_W = 256;
constexpr int B_H = 32, B_DH = 128, B_M = 8, B_SEG = 32, B_NSEG = 64;
constexpr int B_HD = B_H * B_DH;
constexpr int QKVB_LD = 3 * B_HD;
constexpr int QKVA_LD = 3 * D_MODEL;
constexpr int OUT_LD = 2 * D_MODEL;

typedef unsigned short u16;
typedef __attribute__((ext_vector_type(8))) short bf16x8;
typedef __attribute__((ext_vector_type(4))) short bf16x4;
typedef __attribute__((ext_vector_type(4))) float f32x4;
typedef __attribute__((ext_vector_type(4))) unsigned u32x4;
#define GAS __attribute__((address_space(1)))
typedef GAS unsigned gu32;
typedef GAS unsigned long long gu64;
#define RLX_AGENT __ATOMIC_RELAXED, __HIP_MEMORY_SCOPE_AGENT

__device__ __forceinline__ float bf2f(u16 v) { return __uint_as_float(((unsigned)v) << 16); }
__device__ __forceinline__ u16 f2bf(float f) {
  __hip_bfloat16 b = __float2bfloat16(f);
  return *reinterpret_cast<u16*>(&b);
}
__device__ __forceinline__ float rbf(float f) { return bf2f(f2bf(f)); }

__device__ __forceinline__ unsigned boff(unsigned row, unsigned ch) {
  return 256u * row + 16u * (ch ^ (((row & 3) << 2) | ((row >> 2) & 3)));
}

__global__ __launch_bounds__(256, 2) void gemm128(const u16* __restrict__ A, int lda,
                                                  const u16* __restrict__ B, int ldb,
                                                  u16* __restrict__ C, int ldc, int M, int N, int K) {
  __shared__ __attribute__((aligned(16))) char smem[2 * (8192 + 8192)];
  const int nbn = N / 128;
  const int bm = blockIdx.x / nbn, bn = blockIdx.x % nbn;
  const int tid = threadIdx.x, lane = tid & 63, wid = tid >> 6;
  const int wr = wid >> 1, wc = wid & 1;
  const int row0 = bm * 128, col0 = bn * 128;
  u32x4 ra[2], rb[2];
  auto gload = [&](int kt) {
#pragma unroll
    for (int i = 0; i < 2; ++i) {
      int c = tid + 256 * i;
      int r = c >> 2, ch = c & 3;
      ra[i] = *reinterpret_cast<const u32x4*>(A + (size_t)(row0 + r) * lda + kt * 32 + ch * 8);
      int kr = c >> 4, chb = c & 15;
      rb[i] = *reinterpret_cast<const u32x4*>(B + (size_t)(kt * 32 + kr) * ldb + col0 + chb * 8);
    }
  };
  auto swrite = [&](int buf) {
    char* sa = smem + buf * 16384;
    char* sb = sa + 8192;
#pragma unroll
    for (int i = 0; i < 2; ++i) {
      int c = tid + 256 * i;
      int r = c >> 2, ch = c & 3;
      *reinterpret_cast<u32x4*>(sa + r * 64 + ch * 16) = ra[i];
      int kr = c >> 4, chb = c & 15;
      *reinterpret_cast<u32x4*>(sb + boff(kr, chb)) = rb[i];
    }
  };
  f32x4 acc[4][4];
#pragma unroll
  for (int m = 0; m < 4; ++m)
#pragma unroll
    for (int n = 0; n < 4; ++n) acc[m][n] = (f32x4){0.f, 0.f, 0.f, 0.f};
  const int nk = K / 32;
  gload(0);
  swrite(0);
  __syncthreads();
  for (int kt = 0; kt < nk; ++kt) {
    const int buf = kt & 1;
    if (kt + 1 < nk) gload(kt + 1);
    const char* sa = smem + buf * 16384;
    const char* sb = sa + 8192;
    bf16x8 af[4], bfr[4];
#pragma unroll
    for (int m = 0; m < 4; ++m)
      af[m] = *reinterpret_cast<const bf16x8*>(sa + (wr * 64 + m * 16 + (lane & 15)) * 64 + (lane >> 4) * 16);
    {
      const unsigned g = lane >> 4, q = (lane & 15) >> 2, p = lane & 3;
      const unsigned base = (unsigned)(size_t)sb;
#pragma unroll
      for (int n = 0; n < 4; ++n) {
        const unsigned c = wc * 4 + n;
        unsigned a0 = base + boff(8 * g + 0 + q, 2 * c + (p >> 1)) + 8 * (p & 1);
        unsigned a1 = base + boff(8 * g + 4 + q, 2 * c + (p >> 1)) + 8 * (p & 1);
        bf16x4 lo, hi;
        asm volatile("ds_read_b64_tr_b16 %0, %2\n\tds_read_b64_tr_b16 %1, %3\n\ts_waitcnt lgkmcnt(0)"
                     : "=&v"(lo), "=&v"(hi)
                     : "v"(a0), "v"(a1)
                     : "memory");
        bfr[n] = (bf16x8){lo[0], lo[1], lo[2], lo[3], hi[0], hi[1], hi[2], hi[3]};
      }
    }
#pragma unroll
    for (int m = 0; m < 4; ++m)
#pragma unroll
      for (int n = 0; n < 4; ++n)
        acc[m][n] = __builtin_amdgcn_mfma_f32_16x16x32_bf16(af[m], bfr[n], acc[m][n], 0, 0, 0);
    if (kt + 1 < nk) swrite(buf ^ 1);
    __syncthreads();
  }
#pragma unroll
  for (int m = 0; m < 4; ++m)
#pragma unroll
    for (int n = 0; n < 4; ++n)
#pragma unroll
      for (int j = 0; j < 4; ++j)
        C[(size_t)(row0 + wr * 64 + m * 16 + (lane >> 4) * 4 + j) * ldc + col0 + wc * 64 + n * 16 + (lane & 15)] =
            f2bf(acc[m][n][j]);
}

__global__ __launch_bounds__(256, 2) void attnA(const u16* __restrict__ qkv, u16* __restrict__ o) {
  const int h = blockIdx.x % A_H, qb = blockIdx.x / A_H;
  const int tid = threadIdx.x, lane = tid & 63, wid = tid >> 6;
  const int q0 = qb * 64;
  const int kbase = q0 - 256;
  __shared__ __attribute__((aligned(16))) u16 sK[320 * 32];
  __shared__ __attribute__((aligned(16))) u16 sV[320 * 32];
  __shared__ __attribute__((aligned(16))) u16 sP[4][16 * 296];
  for (int c = tid; c < 320 * 4; c += 256) {
    int r = c >> 2, ch = c & 3;
    int key = kbase + r;
    u32x4 kv = (u32x4){0, 0, 0, 0}, vv = (u32x4){0, 0, 0, 0};
    if (key >= 0) {
      kv = *reinterpret_cast<const u32x4*>(qkv + (size_t)key * QKVA_LD + D_MODEL + h * 32 + ch * 8);
      vv = *reinterpret_cast<const u32x4*>(qkv + (size_t)key * QKVA_LD + 2 * D_MODEL + h * 32 + ch * 8);
    }
    *reinterpret_cast<u32x4*>(sK + r * 32 + ch * 8) = kv;
    *reinterpret_cast<u32x4*>(sV + r * 32 + ch * 8) = vv;
  }
  __syncthreads();
  const int qr0 = q0 + wid * 16;
  bf16x8 qf = *reinterpret_cast<const bf16x8*>(qkv + (size_t)(qr0 + (lane & 15)) * QKVA_LD + h * 32 + (lane >> 4) * 8);
  const int t0 = wid;
  f32x4 sc[17];
  const float scale = 0.17677669529663687f;
  float mx[4] = {-3e38f, -3e38f, -3e38f, -3e38f};
#pragma unroll
  for (int t = 0; t < 17; ++t) {
    bf16x8 kf = *reinterpret_cast<const bf16x8*>(sK + ((t0 + t) * 16 + (lane & 15)) * 32 + (lane >> 4) * 8);
    f32x4 s = __builtin_amdgcn_mfma_f32_16x16x32_bf16(qf, kf, (f32x4){0.f, 0.f, 0.f, 0.f}, 0, 0, 0);
    const int key = kbase + (t0 + t) * 16 + (lane & 15);
#pragma unroll
    for (int j = 0; j < 4; ++j) {
      const int row = qr0 + (lane >> 4) * 4 + j;
      float v = rbf(rbf(s[j]) * scale);
      const bool ok = (key <= row) && (row - key < A_W) && (key >= 0);
      v = ok ? v : -1e9f;
      s[j] = v;
      mx[j] = fmaxf(mx[j], v);
    }
    sc[t] = s;
  }
#pragma unroll
  for (int j = 0; j < 4; ++j) {
#pragma unroll
    for (int m = 1; m < 16; m <<= 1) mx[j] = fmaxf(mx[j], __shfl_xor(mx[j], m));
  }
  float sum[4] = {0.f, 0.f, 0.f, 0.f};
#pragma unroll
  for (int t = 0; t < 17; ++t)
#pragma unroll
    for (int j = 0; j < 4; ++j) {
      float e = __expf(sc[t][j] - mx[j]);
      sc[t][j] = e;
      sum[j] += e;
    }
#pragma unroll
  for (int j = 0; j < 4; ++j) {
#pragma unroll
    for (int m = 1; m < 16; m <<= 1) sum[j] += __shfl_xor(sum[j], m);
    sum[j] = 1.f / sum[j];
  }
  u16* P = sP[wid];
#pragma unroll
  for (int t = 0; t < 17; ++t)
#pragma unroll
    for (int j = 0; j < 4; ++j) P[((lane >> 4) * 4 + j) * 296 + t * 16 + (lane & 15)] = f2bf(sc[t][j] * sum[j]);
#pragma unroll
  for (int j = 0; j < 4; ++j) P[((lane >> 4) * 4 + j) * 296 + 272 + (lane & 15)] = 0;
  __syncthreads();
  f32x4 oacc[2] = {(f32x4){0.f, 0.f, 0.f, 0.f}, (f32x4){0.f, 0.f, 0.f, 0.f}};
  for (int ks = 0; ks < 9; ++ks) {
    bf16x8 pf = *reinterpret_cast<const bf16x8*>(P + (lane & 15) * 296 + ks * 32 + (lane >> 4) * 8);
#pragma unroll
    for (int n = 0; n < 2; ++n) {
      bf16x8 vf;
#pragma unroll
      for (int j = 0; j < 8; ++j) {
        int kl = t0 * 16 + ks * 32 + (lane >> 4) * 8 + j;
        kl = kl < 320 ? kl : 319;
        vf[j] = (short)sV[kl * 32 + n * 16 + (lane & 15)];
      }
      oacc[n] = __builtin_amdgcn_mfma_f32_16x16x32_bf16(pf, vf, oacc[n], 0, 0, 0);
    }
  }
#pragma unroll
  for (int n = 0; n < 2; ++n)
#pragma unroll
    for (int j = 0; j < 4; ++j)
      o[(size_t)(qr0 + (lane >> 4) * 4 + j) * D_MODEL + h * 32 + n * 16 + (lane & 15)] = f2bf(oacc[n][j]);
}

__global__ __launch_bounds__(256, 2) void attnBx(const u16* __restrict__ qkv, const u16* __restrict__ kvm,
                                                 u16* __restrict__ o) {
  const int seg = blockIdx.x / 8, hg = blockIdx.x % 8;
  const int tid = threadIdx.x, lane = tid & 63, wid = tid >> 6;
  const int h = hg * 4 + wid;
  __shared__ __attribute__((aligned(16))) u16 sK[4][48 * 136];
  __shared__ __attribute__((aligned(16))) u16 sV[4][64 * 136];
  __shared__ __attribute__((aligned(16))) u16 sP[4][32 * 72];
  u16* K = sK[wid];
  u16* V = sV[wid];
  u16* P = sP[wid];
  for (int c = lane; c < 64 * 16; c += 64) {
    int r = c >> 4, ch = c & 15;
    u32x4 kv = (u32x4){0, 0, 0, 0}, vv = (u32x4){0, 0, 0, 0};
    if (r < 8) {
      kv = *reinterpret_cast<const u32x4*>(kvm + ((size_t)(seg * 2 + 0) * 8 + r) * B_HD + h * 128 + ch * 8);
      vv = *reinterpret_cast<const u32x4*>(kvm + ((size_t)(seg * 2 + 1) * 8 + r) * B_HD + h * 128 + ch * 8);
    } else if (r < 40) {
      const u16* rowp = qkv + (size_t)(seg * 32 + r - 8) * QKVB_LD;
      kv = *reinterpret_cast<const u32x4*>(rowp + B_HD + h * 128 + ch * 8);
      vv = *reinterpret_cast<const u32x4*>(rowp + 2 * B_HD + h * 128 + ch * 8);
    }
    if (r < 48) *reinterpret_cast<u32x4*>(K + r * 136 + ch * 8) = kv;
    *reinterpret_cast<u32x4*>(V + r * 136 + ch * 8) = vv;
  }
  __syncthreads();
  const float scale = 0.08838834764831845f;
#pragma unroll
  for (int mt = 0; mt < 2; ++mt) {
    bf16x8 qf[4];
#pragma unroll
    for (int ks = 0; ks < 4; ++ks)
      qf[ks] = *reinterpret_cast<const bf16x8*>(qkv + (size_t)(seg * 32 + mt * 16 + (lane & 15)) * QKVB_LD + h * 128 +
                                                ks * 32 + (lane >> 4) * 8);
    f32x4 sc[3];
    float mx[4] = {-3e38f, -3e38f, -3e38f, -3e38f};
#pragma unroll
    for (int t = 0; t < 3; ++t) {
      f32x4 s = (f32x4){0.f, 0.f, 0.f, 0.f};
#pragma unroll
      for (int ks = 0; ks < 4; ++ks) {
        bf16x8 kf = *reinterpret_cast<const bf16x8*>(K + (t * 16 + (lane & 15)) * 136 + ks * 32 + (lane >> 4) * 8);
        s = __builtin_amdgcn_mfma_f32_16x16x32_bf16(qf[ks], kf, s, 0, 0, 0);
      }
      const int key = t * 16 + (lane & 15);
#pragma unroll
      for (int j = 0; j < 4; ++j) {
        const int rowp = 8 + mt * 16 + (lane >> 4) * 4 + j;
        float v = rbf(rbf(s[j]) * scale);
        const bool ok = (key <= rowp);
        v = ok ? v : -1e9f;
        s[j] = v;
        mx[j] = fmaxf(mx[j], v);
      }
      sc[t] = s;
    }
#pragma unroll
    for (int j = 0; j < 4; ++j) {
#pragma unroll
      for (int m = 1; m < 16; m <<= 1) mx[j] = fmaxf(mx[j], __shfl_xor(mx[j], m));
    }
    float sum[4] = {0.f, 0.f, 0.f, 0.f};
#pragma unroll
    for (int t = 0; t < 3; ++t)
#pragma unroll
      for (int j = 0; j < 4; ++j) {
        float e = __expf(sc[t][j] - mx[j]);
        sc[t][j] = e;
        sum[j] += e;
      }
#pragma unroll
    for (int j = 0; j < 4; ++j) {
#pragma unroll
      for (int m = 1; m < 16; m <<= 1) sum[j] += __shfl_xor(sum[j], m);
      sum[j] = 1.f / sum[j];
    }
#pragma unroll
    for (int t = 0; t < 3; ++t)
#pragma unroll
      for (int j = 0; j < 4; ++j)
        P[(mt * 16 + (lane >> 4) * 4 + j) * 72 + t * 16 + (lane & 15)] = f2bf(sc[t][j] * sum[j]);
#pragma unroll
    for (int j = 0; j < 4; ++j) P[(mt * 16 + (lane >> 4) * 4 + j) * 72 + 48 + (lane & 15)] = 0;
  }
  __syncthreads();
#pragma unroll
  for (int mt = 0; mt < 2; ++mt) {
    bf16x8 pf[2];
#pragma unroll
    for (int ks = 0; ks < 2; ++ks)
      pf[ks] = *reinterpret_cast<const bf16x8*>(P + (mt * 16 + (lane & 15)) * 72 + ks * 32 + (lane >> 4) * 8);
#pragma unroll
    for (int n = 0; n < 8; ++n) {
      f32x4 oacc = (f32x4){0.f, 0.f, 0.f, 0.f};
#pragma unroll
      for (int ks = 0; ks < 2; ++ks) {
        bf16x8 vf;
#pragma unroll
        for (int j = 0; j < 8; ++j) vf[j] = (short)V[(ks * 32 + (lane >> 4) * 8 + j) * 136 + n * 16 + (lane & 15)];
        oacc = __builtin_amdgcn_mfma_f32_16x16x32_bf16(pf[ks], vf, oacc, 0, 0, 0);
      }
#pragma unroll
      for (int j = 0; j < 4; ++j)
        o[(size_t)(seg * 32 + mt * 16 + (lane >> 4) * 4 + j) * B_HD + h * 128 + n * 16 + (lane & 15)] = f2bf(oacc[j]);
    }
  }
}

constexpr unsigned kSpinCap = 1u << 22;

__device__ __forceinline__ bool spin_fail(unsigned& spins, gu32* tmo, int lane) {
  __builtin_amdgcn_s_sleep(1);
  if (++spins > kSpinCap) {
    if (lane == 0) __hip_atomic_store(tmo, 1u, RLX_AGENT);
    return true;
  }
  return (spins & 255u) == 0u && __builtin_amdgcn_readfirstlane(__hip_atomic_load(tmo, RLX_AGENT));
}
__device__ __forceinline__ void store_granule(gu64* g, unsigned epoch, unsigned value) {
  __hip_atomic_store(g, ((unsigned long long)epoch << 32) | value, RLX_AGENT);
}
template <int N>
__device__ __forceinline__ bool sweep_granules(gu64* g, int S, unsigned epoch, unsigned (&v)[N], gu32* tmo, int lane) {
  for (unsigned spins = 0;;) {
    bool ok = true;
#pragma unroll
    for (int k = 0; k < N; ++k) {
      const unsigned long long x = __hip_atomic_load(g + k * S, RLX_AGENT);
      v[k] = (unsigned)x;
      ok &= (x >> 32) == epoch;
    }
    if (__all(ok)) return true;
    if (spin_fail(spins, tmo, lane)) return false;
  }
}

__device__ __forceinline__ u32x4 load16_sc1(__amdgpu_buffer_rsrc_t rs, unsigned byte_off) {
  return __builtin_amdgcn_raw_buffer_load_b128(rs, byte_off, 0, 16);
}
struct ChainArgs {
  const u16* mem0;
  const u16* Wq;
  const u16* Wk;
  const u16* Wv;
  const u16* Wo;
  const u16* qkvB;
  u16* kvm;
  unsigned long long* gMem;
  unsigned long long* gQK;
  unsigned long long* gO;
  unsigned* tmo;
};

#define STAMP(idx) do {} while (0)
constexpr int MEM_LDS_STRIDE = 1032;

__global__ __launch_bounds__(256, 1) void chain_kernel(ChainArgs a) {
  extern __shared__ __attribute__((aligned(16))) char lds[];
  const int b = blockIdx.x;
  const int tid = threadIdx.x, lane = tid & 63;
  const int w = __builtin_amdgcn_readfirstlane(tid >> 6);
  gu64* gMem = (gu64*)a.gMem;
  gu64* gQK = (gu64*)a.gQK;
  gu64* gO = (gu64*)a.gO;
  gu32* tmo = (gu32*)a.tmo;
  const __amdgpu_buffer_rsrc_t rsMem = __builtin_amdgcn_make_buffer_rsrc((void*)a.gMem, 0, 32768, 0x00020000);
  const __amdgpu_buffer_rsrc_t rsO = __builtin_amdgcn_make_buffer_rsrc((void*)a.gO, 0, 131072, 0x00020000);

  if (b < 192) {
    const int t = b / 64, cb = b % 64;
    const u16* W = t == 0 ? a.Wq : (t == 1 ? a.Wk : a.Wv);
    const int col = cb * 64 + w * 16 + (lane & 15);
    bf16x8 wf[32];
#pragma unroll
    for (int i = 0; i < 32; ++i) {
#pragma unroll
      for (int j = 0; j < 8; ++j) wf[i][j] = (short)W[(size_t)(i * 32 + (lane >> 4) * 8 + j) * B_HD + col];
      asm volatile("" : "+v"(wf[i]));
    }
    u16* sMem = (u16*)lds;
    u16* sQ = (u16*)(lds + 16896);
    u16* sK = sQ + 8 * 136;
    u16* sVT = sK + 48 * 136;
    if (t == 2) {
      for (int i = tid; i < 8 * 136; i += 256) sK[40 * 136 + i] = 0;
      for (int i = tid; i < 64 * 8; i += 256) sVT[(i >> 3) * 56 + 40 + (i & 7)] = 0;
    }
    const int h = cb >> 1, half = cb & 1;
    for (int s = 0; s < B_NSEG; ++s) {
      const unsigned epoch = (unsigned)s + 1u;
      u32x4 kxr0 = (u32x4){0, 0, 0, 0}, kxr1 = (u32x4){0, 0, 0, 0}, vxr = (u32x4){0, 0, 0, 0};
      if (t == 2) {
        {
          int r = tid >> 4, ch = tid & 15;
          kxr0 = *reinterpret_cast<const u32x4*>(a.qkvB + (size_t)(s * 32 + r) * QKVB_LD + B_HD + h * 128 + ch * 8);
          kxr1 = *reinterpret_cast<const u32x4*>(a.qkvB + (size_t)(s * 32 + 16 + r) * QKVB_LD + B_HD + h * 128 + ch * 8);
        }
        {
          int r = tid >> 3, ch = tid & 7;
          vxr = *reinterpret_cast<const u32x4*>(a.qkvB + (size_t)(s * 32 + r) * QKVB_LD + 2 * B_HD + h * 128 +
                                                half * 64 + ch * 8);
        }
      }
      STAMP(0);
      if (s == 0) {
#pragma unroll
        for (int k = 0; k < 16; ++k) {
          int g = tid + 256 * k;
          int r = g >> 9, cp = g & 511;
          unsigned v = *reinterpret_cast<const unsigned*>(a.mem0 + r * 1024 + cp * 2);
          *reinterpret_cast<unsigned*>(sMem + r * MEM_LDS_STRIDE + cp * 2) = v;
        }
      } else {
        if (w == 0) {
          if (t < 2) {
            __builtin_amdgcn_s_sleep(127);
          } else {
            __builtin_amdgcn_s_sleep(48);
          }
          unsigned spins = 0;
          for (;;) {
            const unsigned long long x = __hip_atomic_load(gMem + 7 * 512 + lane * 8 + 7, RLX_AGENT);
            if (__all((x >> 32) == (unsigned)s)) break;
            if (spin_fail(spins, tmo, lane)) break;
          }
        }
        __syncthreads();
        {
          u32x4 x[8];
          unsigned spins = 0;
          for (;;) {
#pragma unroll
            for (int k = 0; k < 8; ++k) x[k] = load16_sc1(rsMem, 16u * (tid + 256 * k));
            bool ok = true;
#pragma unroll
            for (int k = 0; k < 8; ++k) ok &= (x[k][1] == (unsigned)s) & (x[k][3] == (unsigned)s);
            if (__all(ok)) break;
            if (spin_fail(spins, tmo, lane)) return;
          }
#pragma unroll
          for (int k = 0; k < 8; ++k) {
            int g = 2 * (tid + 256 * k);
            int r = g >> 9, cp = g & 511;
            *reinterpret_cast<unsigned*>(sMem + r * MEM_LDS_STRIDE + cp * 2) = x[k][0];
            *reinterpret_cast<unsigned*>(sMem + r * MEM_LDS_STRIDE + cp * 2 + 2) = x[k][2];
          }
        }
      }
      __syncthreads();
      STAMP(1);
      f32x4 acc = (f32x4){0.f, 0.f, 0.f, 0.f}, acc2 = (f32x4){0.f, 0.f, 0.f, 0.f};
      {
        bf16x8 af[32];
#pragma unroll
        for (int i = 0; i < 32; ++i)
          af[i] = *reinterpret_cast<const bf16x8*>(sMem + (lane & 7) * MEM_LDS_STRIDE + i * 32 + (lane >> 4) * 8);
#pragma unroll
        for (int i = 0; i < 32; i += 2) {
          acc = __builtin_amdgcn_mfma_f32_16x16x32_bf16(wf[i], af[i], acc, 0, 0, 0);
          acc2 = __builtin_amdgcn_mfma_f32_16x16x32_bf16(wf[i + 1], af[i + 1], acc2, 0, 0, 0);
        }
        acc += acc2;
      }
      {
        if ((lane & 15) < 8) {
          const int r = lane & 15;
#pragma unroll
          for (int jj = 0; jj < 2; ++jj) {
            const unsigned pk = (unsigned)f2bf(acc[2 * jj]) | ((unsigned)f2bf(acc[2 * jj + 1]) << 16);
            const int cp = w * 8 + (lane >> 4) * 2 + jj;
            if (t < 2) store_granule(gQK + (size_t)t * 16384 + r * 2048 + cb * 32 + cp, epoch, pk);
            if (t >= 1)
              *reinterpret_cast<unsigned*>(a.kvm + ((size_t)(s * 2 + (t - 1)) * 8 + r) * B_HD + cb * 64 + cp * 2) = pk;
            if (t == 2) {
              sVT[(cp * 2) * 56 + r] = (u16)(pk & 0xffff);
              sVT[(cp * 2 + 1) * 56 + r] = (u16)(pk >> 16);
            }
          }
        }
      }
      STAMP(2);
      if (t == 2) {
        {
          int r = tid >> 4, ch = tid & 15;
          *reinterpret_cast<u32x4*>(sK + (8 + r) * 136 + ch * 8) = kxr0;
          *reinterpret_cast<u32x4*>(sK + (24 + r) * 136 + ch * 8) = kxr1;
        }
        {
          int r = tid >> 3, ch = tid & 7;
#pragma unroll
          for (int e = 0; e < 4; ++e) {
            sVT[(ch * 8 + 2 * e) * 56 + 8 + r] = (u16)(vxr[e] & 0xffff);
            sVT[(ch * 8 + 2 * e + 1) * 56 + 8 + r] = (u16)(vxr[e] >> 16);
          }
        }
        if (w == 0) {
          unsigned spins = 0;
          for (;;) {
            const unsigned long long x = __hip_atomic_load(
                gQK + (size_t)((lane >> 1) & 1) * 16384 + 7 * 2048 + h * 64 + (lane & 1) * 32 + 31, RLX_AGENT);
            if (__all((x >> 32) == epoch)) break;
            if (spin_fail(spins, tmo, lane)) break;
          }
        }
        __syncthreads();
        {
          unsigned v[4];
          int r = tid >> 5, cp = tid & 31;
          {
            unsigned spins = 0;
            for (;;) {
              bool ok = true;
#pragma unroll
              for (int k = 0; k < 4; ++k) {
                const unsigned long long x = __hip_atomic_load(
                    gQK + (size_t)(k >> 1) * 16384 + r * 2048 + h * 64 + (k & 1) * 32 + cp, RLX_AGENT);
                v[k] = (unsigned)x;
                ok &= (x >> 32) == epoch;
              }
              if (__all(ok)) break;
              if (spin_fail(spins, tmo, lane)) return;
            }
          }
          *reinterpret_cast<unsigned*>(sQ + r * 136 + cp * 2) = v[0];
          *reinterpret_cast<unsigned*>(sQ + r * 136 + 64 + cp * 2) = v[1];
          *reinterpret_cast<unsigned*>(sK + r * 136 + cp * 2) = v[2];
          *reinterpret_cast<unsigned*>(sK + r * 136 + 64 + cp * 2) = v[3];
        }
        __syncthreads();
        STAMP(3);
        if (w == 0) {
          bf16x8 qf[4], kf[3][4];
#pragma unroll
          for (int ks = 0; ks < 4; ++ks)
            qf[ks] = *reinterpret_cast<const bf16x8*>(sQ + (lane & 7) * 136 + ks * 32 + (lane >> 4) * 8);
#pragma unroll
          for (int tt = 0; tt < 3; ++tt)
#pragma unroll
            for (int ks = 0; ks < 4; ++ks)
              kf[tt][ks] = *reinterpret_cast<const bf16x8*>(sK + (tt * 16 + (lane & 15)) * 136 + ks * 32 + (lane >> 4) * 8);
          bf16x4 vlo[4], vmid[4], vhi[4];
#pragma unroll
          for (int n = 0; n < 4; ++n) {
            const u16* vp = sVT + (n * 16 + (lane & 15)) * 56 + (lane >> 4) * 4;
            vlo[n] = *reinterpret_cast<const bf16x4*>(vp);
            vmid[n] = *reinterpret_cast<const bf16x4*>(vp + 16);
            vhi[n] = *reinterpret_cast<const bf16x4*>(vp + 32);
          }
          f32x4 sc[3];
          float mx = -3e38f;
#pragma unroll
          for (int tt = 0; tt < 3; ++tt) {
            f32x4 sv = (f32x4){0.f, 0.f, 0.f, 0.f};
#pragma unroll
            for (int ks = 0; ks < 4; ++ks) sv = __builtin_amdgcn_mfma_f32_16x16x32_bf16(kf[tt][ks], qf[ks], sv, 0, 0, 0);
#pragma unroll
            for (int j = 0; j < 4; ++j) {
              const int key = tt * 16 + (lane >> 4) * 4 + j;
              float v = rbf(rbf(sv[j]) * 0.08838834764831845f);
              v = key < 40 ? v : -3e38f;
              sv[j] = v;
              mx = fmaxf(mx, v);
            }
            sc[tt] = sv;
          }
          mx = fmaxf(mx, __shfl_xor(mx, 16));
          mx = fmaxf(mx, __shfl_xor(mx, 32));
          float sum = 0.f;
#pragma unroll
          for (int tt = 0; tt < 3; ++tt)
#pragma unroll
            for (int j = 0; j < 4; ++j) {
              float e = __expf(sc[tt][j] - mx);
              sc[tt][j] = e;
              sum += e;
            }
          sum += __shfl_xor(sum, 16);
          sum += __shfl_xor(sum, 32);
          const float inv = 1.f / sum;
          bf16x8 p0, p1;
#pragma unroll
          for (int j = 0; j < 4; ++j) {
            p0[j] = (short)f2bf(sc[0][j] * inv);
            p0[4 + j] = (short)f2bf(sc[1][j] * inv);
            p1[j] = (short)f2bf(sc[2][j] * inv);
            p1[4 + j] = 0;
          }
#pragma unroll
          for (int n = 0; n < 4; ++n) {
            bf16x8 va = (bf16x8){vlo[n][0], vlo[n][1], vlo[n][2], vlo[n][3], vmid[n][0], vmid[n][1], vmid[n][2], vmid[n][3]};
            bf16x8 vb = (bf16x8){vhi[n][0], vhi[n][1], vhi[n][2], vhi[n][3], 0, 0, 0, 0};
            f32x4 oacc = __builtin_amdgcn_mfma_f32_16x16x32_bf16(va, p0, (f32x4){0.f, 0.f, 0.f, 0.f}, 0, 0, 0);
            oacc = __builtin_amdgcn_mfma_f32_16x16x32_bf16(vb, p1, oacc, 0, 0, 0);
            if ((lane & 15) < 8) {
#pragma unroll
              for (int jj = 0; jj < 2; ++jj)
                store_granule(gO + (lane & 15) * 2048 + cb * 32 + n * 8 + (lane >> 4) * 2 + jj, epoch,
                              (unsigned)f2bf(oacc[2 * jj]) | ((unsigned)f2bf(oacc[2 * jj + 1]) << 16));
            }
          }
          STAMP(4);
        }
      }
      __syncthreads();
    }
  } else {
    const int ob = b - 192;
    const int col = ob * 16 + (lane & 15);
    bf16x8 wf[32];
#pragma unroll
    for (int i = 0; i < 32; ++i) {
#pragma unroll
      for (int j = 0; j < 8; ++j)
        wf[i][j] = (short)a.Wo[(size_t)(w * 1024 + i * 32 + (lane >> 4) * 8 + j) * D_MODEL + col];
      asm volatile("" : "+v"(wf[i]));
    }
    u16* sO = (u16*)lds + w * (8 * 1032);
    float* sRed = (float*)(lds + 4 * 8 * 1032 * 2);
    for (int s = 0; s < B_NSEG; ++s) {
      const unsigned epoch = (unsigned)s + 1u;
      STAMP(0);
      if (s > 0) __builtin_amdgcn_s_sleep(100);
      {
        unsigned spins = 0;
        for (;;) {
          const unsigned long long x = __hip_atomic_load(gO + 7 * 2048 + w * 512 + (lane & 15) * 32 + 31, RLX_AGENT);
          bool ok = (x >> 32) == epoch;
          if (__all(ok)) break;
          if (spin_fail(spins, tmo, lane)) return;
        }
      }
      {
        u32x4 x[32];
        unsigned spins = 0;
        for (;;) {
#pragma unroll
          for (int k = 0; k < 32; ++k) x[k] = load16_sc1(rsO, 8u * ((k >> 2) * 2048 + w * 512 + ((k & 3) * 64 + lane) * 2));
          bool ok = true;
#pragma unroll
          for (int k = 0; k < 32; ++k) ok &= (x[k][1] == epoch) & (x[k][3] == epoch);
          if (__all(ok)) break;
          if (spin_fail(spins, tmo, lane)) return;
        }
#pragma unroll
        for (int k = 0; k < 32; ++k) {
          u16* d = sO + (k >> 2) * 1032 + ((k & 3) * 64 + lane) * 4;
          *reinterpret_cast<unsigned*>(d) = x[k][0];
          *reinterpret_cast<unsigned*>(d + 2) = x[k][2];
        }
      }
      __builtin_amdgcn_s_waitcnt(0xc07f);
      STAMP(1);
      f32x4 acc = (f32x4){0.f, 0.f, 0.f, 0.f}, acc2 = (f32x4){0.f, 0.f, 0.f, 0.f};
      {
        bf16x8 af[32];
#pragma unroll
        for (int i = 0; i < 32; ++i)
          af[i] = *reinterpret_cast<const bf16x8*>(sO + (lane & 7) * 1032 + i * 32 + (lane >> 4) * 8);
#pragma unroll
        for (int i = 0; i < 32; i += 2) {
          acc = __builtin_amdgcn_mfma_f32_16x16x32_bf16(wf[i], af[i], acc, 0, 0, 0);
          acc2 = __builtin_amdgcn_mfma_f32_16x16x32_bf16(wf[i + 1], af[i + 1], acc2, 0, 0, 0);
        }
        acc += acc2;
      }
      if ((lane & 15) < 8) {
#pragma unroll
        for (int j = 0; j < 4; ++j) sRed[(w * 8 + (lane & 15)) * 16 + (lane >> 4) * 4 + j] = acc[j];
      }
      __syncthreads();
      if (tid < 64) {
        int r = tid >> 3, cp = tid & 7;
        float v0 = 0.f, v1 = 0.f;
#pragma unroll
        for (int q = 0; q < 4; ++q) {
          v0 += sRed[(q * 8 + r) * 16 + cp * 2];
          v1 += sRed[(q * 8 + r) * 16 + cp * 2 + 1];
        }
        unsigned pk = (unsigned)f2bf(v0) | ((unsigned)f2bf(v1) << 16);
        if (s + 1 < B_NSEG) store_granule(gMem + r * 512 + ob * 8 + cp, epoch, pk);
      }
      STAMP(2);
      __syncthreads();
    }
  }
}

extern "C" void kernel_launch(void* const* d_in, const int* in_sizes, int n_in, void* d_out, int out_size, void* d_ws,
                              size_t ws_size, hipStream_t stream) {
  const u16* A_x = (const u16*)d_in[0];
  const u16* A_wqkv = (const u16*)d_in[1];
  const u16* A_wo = (const u16*)d_in[2];
  const u16* B_x = (const u16*)d_in[3];
  const u16* B_mem0 = (const u16*)d_in[4];
  const u16* B_Wq = (const u16*)d_in[5];
  const u16* B_Wk = (const u16*)d_in[6];
  const u16* B_Wv = (const u16*)d_in[7];
  const u16* B_Wo = (const u16*)d_in[8];
  u16* out = (u16*)d_out;
  char* ws = (char*)d_ws;
  u16* qkvB = (u16*)ws;
  u16* qkvA = (u16*)(ws + (size_t)50331648);
  u16* oA = (u16*)(ws + (size_t)50331648 + 12582912);
  u16* kvm = (u16*)(ws + (size_t)67108864);
  u16* oB = (u16*)(ws + (size_t)75497472);
  char* gran = ws + (size_t)92274688;
  const size_t gran_bytes = 32768 + 262144 + 131072 + 256;
  (void)hipMemsetAsync(gran, 0, gran_bytes, stream);

  gemm128<<<(2048 / 128) * (4096 / 128), 256, 0, stream>>>(B_x, 1024, B_Wq, 4096, qkvB, QKVB_LD, 2048, 4096, 1024);
  gemm128<<<(2048 / 128) * (4096 / 128), 256, 0, stream>>>(B_x, 1024, B_Wk, 4096, qkvB + 4096, QKVB_LD, 2048, 4096, 1024);
  gemm128<<<(2048 / 128) * (4096 / 128), 256, 0, stream>>>(B_x, 1024, B_Wv, 4096, qkvB + 8192, QKVB_LD, 2048, 4096, 1024);
  ChainArgs ca;
  ca.mem0 = B_mem0;
  ca.Wq = B_Wq;
  ca.Wk = B_Wk;
  ca.Wv = B_Wv;
  ca.Wo = B_Wo;
  ca.qkvB = qkvB;
  ca.kvm = kvm;
  ca.gMem = (unsigned long long*)gran;
  ca.gQK = (unsigned long long*)(gran + 32768);
  ca.gO = (unsigned long long*)(gran + 32768 + 262144);
  ca.tmo = (unsigned*)(gran + 32768 + 262144 + 131072);
  (void)hipFuncSetAttribute((const void*)chain_kernel, hipFuncAttributeMaxDynamicSharedMemorySize, 96 * 1024);
  chain_kernel<<<256, 256, 96 * 1024, stream>>>(ca);
  attnBx<<<B_NSEG * 8, 256, 0, stream>>>(qkvB, kvm, oB);
  gemm128<<<(2048 / 128) * (1024 / 128), 256, 0, stream>>>(oB, 4096, B_Wo, 1024, out + 1024, OUT_LD, 2048, 1024, 4096);
  gemm128<<<(2048 / 128) * (3072 / 128), 256, 0, stream>>>(A_x, 1024, A_wqkv, 3072, qkvA, QKVA_LD, 2048, 3072, 1024);
  attnA<<<(2048 / 64) * A_H, 256, 0, stream>>>(qkvA, oA);
  gemm128<<<(2048 / 128) * (1024 / 128), 256, 0, stream>>>(oA, 1024, A_wo, 1024, out, OUT_LD, 2048, 1024, 1024);
}
```

```cpp
#include <hip/hip_runtime.h>
#include <hip/hip_bf16.h>
#include <stdint.h>

constexpr int S_LEN = 2048;
constexpr int D_MODEL = 1024;
constexpr int A_H = 32, A_DH = 32, A_W = 256;
constexpr int B_H = 32, B_DH = 128, B_M = 8, B_SEG = 32, B_NSEG = 64;
constexpr int B_HD = B_H * B_DH;
constexpr int QKVB_LD = 3 * B_HD;
constexpr int QKVA_LD = 3 * D_MODEL;
constexpr int OUT_LD = 2 * D_MODEL;

typedef unsigned short u16;
typedef __attribute__((ext_vector_type(8))) short bf16x8;
typedef __attribute__((ext_vector_type(4))) short bf16x4;
typedef __attribute__((ext_vector_type(4))) float f32x4;
typedef __attribute__((ext_vector_type(4))) unsigned u32x4;
#define GAS __attribute__((address_space(1)))
typedef GAS unsigned gu32;
typedef GAS unsigned long long gu64;
#define RLX_AGENT __ATOMIC_RELAXED, __HIP_MEMORY_SCOPE_AGENT

__device__ __forceinline__ float bf2f(u16 v) { return __uint_as_float(((unsigned)v) << 16); }
__device__ __forceinline__ u16 f2bf(float f) {
  __hip_bfloat16 b = __float2bfloat16(f);
  return *reinterpret_cast<u16*>(&b);
}
__device__ __forceinline__ float rbf(float f) { return bf2f(f2bf(f)); }

__device__ __forceinline__ unsigned boff(unsigned row, unsigned ch) {
  return 256u * row + 16u * (ch ^ (((row & 3) << 2) | ((row >> 2) & 3)));
}

__global__ __launch_bounds__(256, 2) void gemm128(const u16* __restrict__ A, int lda,
                                                  const u16* __restrict__ B, int ldb,
                                                  u16* __restrict__ C, int ldc, int M, int N, int K) {
  __shared__ __attribute__((aligned(16))) char smem[2 * (8192 + 8192)];
  const int nbn = N / 128;
  const int bm = blockIdx.x / nbn, bn = blockIdx.x % nbn;
  const int tid = threadIdx.x, lane = tid & 63, wid = tid >> 6;
  const int wr = wid >> 1, wc = wid & 1;
  const int row0 = bm * 128, col0 = bn * 128;
  u32x4 ra[2], rb[2];
  auto gload = [&](int kt) {
#pragma unroll
    for (int i = 0; i < 2; ++i) {
      int c = tid + 256 * i;
      int r = c >> 2, ch = c & 3;
      ra[i] = *reinterpret_cast<const u32x4*>(A + (size_t)(row0 + r) * lda + kt * 32 + ch * 8);
      int kr = c >> 4, chb = c & 15;
      rb[i] = *reinterpret_cast<const u32x4*>(B + (size_t)(kt * 32 + kr) * ldb + col0 + chb * 8);
    }
  };
  auto swrite = [&](int buf) {
    char* sa = smem + buf * 16384;
    char* sb = sa + 8192;
#pragma unroll
    for (int i = 0; i < 2; ++i) {
      int c = tid + 256 * i;
      int r = c >> 2, ch = c & 3;
      *reinterpret_cast<u32x4*>(sa + r * 64 + ch * 16) = ra[i];
      int kr = c >> 4, chb = c & 15;
      *reinterpret_cast<u32x4*>(sb + boff(kr, chb)) = rb[i];
    }
  };
  f32x4 acc[4][4];
#pragma unroll
  for (int m = 0; m < 4; ++m)
#pragma unroll
    for (int n = 0; n < 4; ++n) acc[m][n] = (f32x4){0.f, 0.f, 0.f, 0.f};
  const int nk = K / 32;
  gload(0);
  swrite(0);
  __syncthreads();
  for (int kt = 0; kt < nk; ++kt) {
    const int buf = kt & 1;
    if (kt + 1 < nk) gload(kt + 1);
    const char* sa = smem + buf * 16384;
    const char* sb = sa + 8192;
    bf16x8 af[4], bfr[4];
#pragma unroll
    for (int m = 0; m < 4; ++m)
      af[m] = *reinterpret_cast<const bf16x8*>(sa + (wr * 64 + m * 16 + (lane & 15)) * 64 + (lane >> 4) * 16);
    {
      const unsigned g = lane >> 4, q = (lane & 15) >> 2, p = lane & 3;
      const unsigned base = (unsigned)(size_t)sb;
#pragma unroll
      for (int n = 0; n < 4; ++n) {
        const unsigned c = wc * 4 + n;
        unsigned a0 = base + boff(8 * g + 0 + q, 2 * c + (p >> 1)) + 8 * (p & 1);
        unsigned a1 = base + boff(8 * g + 4 + q, 2 * c + (p >> 1)) + 8 * (p & 1);
        bf16x4 lo, hi;
        asm volatile("ds_read_b64_tr_b16 %0, %2\n\tds_read_b64_tr_b16 %1, %3\n\ts_waitcnt lgkmcnt(0)"
                     : "=&v"(lo), "=&v"(hi)
                     : "v"(a0), "v"(a1)
                     : "memory");
        bfr[n] = (bf16x8){lo[0], lo[1], lo[2], lo[3], hi[0], hi[1], hi[2], hi[3]};
      }
    }
#pragma unroll
    for (int m = 0; m < 4; ++m)
#pragma unroll
      for (int n = 0; n < 4; ++n)
        acc[m][n] = __builtin_amdgcn_mfma_f32_16x16x32_bf16(af[m], bfr[n], acc[m][n], 0, 0, 0);
    if (kt + 1 < nk) swrite(buf ^ 1);
    __syncthreads();
  }
#pragma unroll
  for (int m = 0; m < 4; ++m)
#pragma unroll
    for (int n = 0; n < 4; ++n)
#pragma unroll
      for (int j = 0; j < 4; ++j)
        C[(size_t)(row0 + wr * 64 + m * 16 + (lane >> 4) * 4 + j) * ldc + col0 + wc * 64 + n * 16 + (lane & 15)] =
            f2bf(acc[m][n][j]);
}

__global__ __launch_bounds__(256, 2) void attnA(const u16* __restrict__ qkv, u16* __restrict__ o) {
  const int h = blockIdx.x % A_H, qb = blockIdx.x / A_H;
  const int tid = threadIdx.x, lane = tid & 63, wid = tid >> 6;
  const int q0 = qb * 64;
  const int kbase = q0 - 256;
  __shared__ __attribute__((aligned(16))) u16 sK[320 * 32];
  __shared__ __attribute__((aligned(16))) u16 sV[320 * 32];
  __shared__ __attribute__((aligned(16))) u16 sP[4][16 * 296];
  for (int c = tid; c < 320 * 4; c += 256) {
    int r = c >> 2, ch = c & 3;
    int key = kbase + r;
    u32x4 kv = (u32x4){0, 0, 0, 0}, vv = (u32x4){0, 0, 0, 0};
    if (key >= 0) {
      kv = *reinterpret_cast<const u32x4*>(qkv + (size_t)key * QKVA_LD + D_MODEL + h * 32 + ch * 8);
      vv = *reinterpret_cast<const u32x4*>(qkv + (size_t)key * QKVA_LD + 2 * D_MODEL + h * 32 + ch * 8);
    }
    *reinterpret_cast<u32x4*>(sK + r * 32 + ch * 8) = kv;
    *reinterpret_cast<u32x4*>(sV + r * 32 + ch * 8) = vv;
  }
  __syncthreads();
  const int qr0 = q0 + wid * 16;
  bf16x8 qf = *reinterpret_cast<const bf16x8*>(qkv + (size_t)(qr0 + (lane & 15)) * QKVA_LD + h * 32 + (lane >> 4) * 8);
  const int t0 = wid;
  f32x4 sc[17];
  const float scale = 0.17677669529663687f;
  float mx[4] = {-3e38f, -3e38f, -3e38f, -3e38f};
#pragma unroll
  for (int t = 0; t < 17; ++t) {
    bf16x8 kf = *reinterpret_cast<const bf16x8*>(sK + ((t0 + t) * 16 + (lane & 15)) * 32 + (lane >> 4) * 8);
    f32x4 s = __builtin_amdgcn_mfma_f32_16x16x32_bf16(qf, kf, (f32x4){0.f, 0.f, 0.f, 0.f}, 0, 0, 0);
    const int key = kbase + (t0 + t) * 16 + (lane & 15);
#pragma unroll
    for (int j = 0; j < 4; ++j) {
      const int row = qr0 + (lane >> 4) * 4 + j;
      float v = rbf(rbf(s[j]) * scale);
      const bool ok = (key <= row) && (row - key < A_W) && (key >= 0);
      v = ok ? v : -1e9f;
      s[j] = v;
      mx[j] = fmaxf(mx[j], v);
    }
    sc[t] = s;
  }
#pragma unroll
  for (int j = 0; j < 4; ++j) {
#pragma unroll
    for (int m = 1; m < 16; m <<= 1) mx[j] = fmaxf(mx[j], __shfl_xor(mx[j], m));
  }
  float sum[4] = {0.f, 0.f, 0.f, 0.f};
#pragma unroll
  for (int t = 0; t < 17; ++t)
#pragma unroll
    for (int j = 0; j < 4; ++j) {
      float e = __expf(sc[t][j] - mx[j]);
      sc[t][j] = e;
      sum[j] += e;
    }
#pragma unroll
  for (int j = 0; j < 4; ++j) {
#pragma unroll
    for (int m = 1; m < 16; m <<= 1) sum[j] += __shfl_xor(sum[j], m);
    sum[j] = 1.f / sum[j];
  }
  u16* P = sP[wid];
#pragma unroll
  for (int t = 0; t < 17; ++t)
#pragma unroll
    for (int j = 0; j < 4; ++j) P[((lane >> 4) * 4 + j) * 296 + t * 16 + (lane & 15)] = f2bf(sc[t][j] * sum[j]);
#pragma unroll
  for (int j = 0; j < 4; ++j) P[((lane >> 4) * 4 + j) * 296 + 272 + (lane & 15)] = 0;
  __syncthreads();
  f32x4 oacc[2] = {(f32x4){0.f, 0.f, 0.f, 0.f}, (f32x4){0.f, 0.f, 0.f, 0.f}};
  for (int ks = 0; ks < 9; ++ks) {
    bf16x8 pf = *reinterpret_cast<const bf16x8*>(P + (lane & 15) * 296 + ks * 32 + (lane >> 4) * 8);
#pragma unroll
    for (int n = 0; n < 2; ++n) {
      bf16x8 vf;
#pragma unroll
      for (int j = 0; j < 8; ++j) {
        int kl = t0 * 16 + ks * 32 + (lane >> 4) * 8 + j;
        kl = kl < 320 ? kl : 319;
        vf[j] = (short)sV[kl * 32 + n * 16 + (lane & 15)];
      }
      oacc[n] = __builtin_amdgcn_mfma_f32_16x16x32_bf16(pf, vf, oacc[n], 0, 0, 0);
    }
  }
#pragma unroll
  for (int n = 0; n < 2; ++n)
#pragma unroll
    for (int j = 0; j < 4; ++j)
      o[(size_t)(qr0 + (lane >> 4) * 4 + j) * D_MODEL + h * 32 + n * 16 + (lane & 15)] = f2bf(oacc[n][j]);
}

__global__ __launch_bounds__(256, 2) void attnBx(const u16* __restrict__ qkv, const u16* __restrict__ kvm,
                                                 u16* __restrict__ o) {
  const int seg = blockIdx.x / 8, hg = blockIdx.x % 8;
  const int tid = threadIdx.x, lane = tid & 63, wid = tid >> 6;
  const int h = hg * 4 + wid;
  __shared__ __attribute__((aligned(16))) u16 sK[4][48 * 136];
  __shared__ __attribute__((aligned(16))) u16 sV[4][64 * 136];
  __shared__ __attribute__((aligned(16))) u16 sP[4][32 * 72];
  u16* K = sK[wid];
  u16* V = sV[wid];
  u16* P = sP[wid];
  for (int c = lane; c < 64 * 16; c += 64) {
    int r = c >> 4, ch = c & 15;
    u32x4 kv = (u32x4){0, 0, 0, 0}, vv = (u32x4){0, 0, 0, 0};
    if (r < 8) {
      kv = *reinterpret_cast<const u32x4*>(kvm + ((size_t)(seg * 2 + 0) * 8 + r) * B_HD + h * 128 + ch * 8);
      vv = *reinterpret_cast<const u32x4*>(kvm + ((size_t)(seg * 2 + 1) * 8 + r) * B_HD + h * 128 + ch * 8);
    } else if (r < 40) {
      const u16* rowp = qkv + (size_t)(seg * 32 + r - 8) * QKVB_LD;
      kv = *reinterpret_cast<const u32x4*>(rowp + B_HD + h * 128 + ch * 8);
      vv = *reinterpret_cast<const u32x4*>(rowp + 2 * B_HD + h * 128 + ch * 8);
    }
    if (r < 48) *reinterpret_cast<u32x4*>(K + r * 136 + ch * 8) = kv;
    *reinterpret_cast<u32x4*>(V + r * 136 + ch * 8) = vv;
  }
  __syncthreads();
  const float scale = 0.08838834764831845f;
#pragma unroll
  for (int mt = 0; mt < 2; ++mt) {
    bf16x8 qf[4];
#pragma unroll
    for (int ks = 0; ks < 4; ++ks)
      qf[ks] = *reinterpret_cast<const bf16x8*>(qkv + (size_t)(seg * 32 + mt * 16 + (lane & 15)) * QKVB_LD + h * 128 +
                                                ks * 32 + (lane >> 4) * 8);
    f32x4 sc[3];
    float mx[4] = {-3e38f, -3e38f, -3e38f, -3e38f};
#pragma unroll
    for (int t = 0; t < 3; ++t) {
      f32x4 s = (f32x4){0.f, 0.f, 0.f, 0.f};
#pragma unroll
      for (int ks = 0; ks < 4; ++ks) {
        bf16x8 kf = *reinterpret_cast<const bf16x8*>(K + (t * 16 + (lane & 15)) * 136 + ks * 32 + (lane >> 4) * 8);
        s = __builtin_amdgcn_mfma_f32_16x16x32_bf16(qf[ks], kf, s, 0, 0, 0);
      }
      const int key = t * 16 + (lane & 15);
#pragma unroll
      for (int j = 0; j < 4; ++j) {
        const int rowp = 8 + mt * 16 + (lane >> 4) * 4 + j;
        float v = rbf(rbf(s[j]) * scale);
        const bool ok = (key <= rowp);
        v = ok ? v : -1e9f;
        s[j] = v;
        mx[j] = fmaxf(mx[j], v);
      }
      sc[t] = s;
    }
#pragma unroll
    for (int j = 0; j < 4; ++j) {
#pragma unroll
      for (int m = 1; m < 16; m <<= 1) mx[j] = fmaxf(mx[j], __shfl_xor(mx[j], m));
    }
    float sum[4] = {0.f, 0.f, 0.f, 0.f};
#pragma unroll
    for (int t = 0; t < 3; ++t)
#pragma unroll
      for (int j = 0; j < 4; ++j) {
        float e = __expf(sc[t][j] - mx[j]);
        sc[t][j] = e;
        sum[j] += e;
      }
#pragma unroll
    for (int j = 0; j < 4; ++j) {
#pragma unroll
      for (int m = 1; m < 16; m <<= 1) sum[j] += __shfl_xor(sum[j], m);
      sum[j] = 1.f / sum[j];
    }
#pragma unroll
    for (int t = 0; t < 3; ++t)
#pragma unroll
      for (int j = 0; j < 4; ++j)
        P[(mt * 16 + (lane >> 4) * 4 + j) * 72 + t * 16 + (lane & 15)] = f2bf(sc[t][j] * sum[j]);
#pragma unroll
    for (int j = 0; j < 4; ++j) P[(mt * 16 + (lane >> 4) * 4 + j) * 72 + 48 + (lane & 15)] = 0;
  }
  __syncthreads();
#pragma unroll
  for (int mt = 0; mt < 2; ++mt) {
    bf16x8 pf[2];
#pragma unroll
    for (int ks = 0; ks < 2; ++ks)
      pf[ks] = *reinterpret_cast<const bf16x8*>(P + (mt * 16 + (lane & 15)) * 72 + ks * 32 + (lane >> 4) * 8);
#pragma unroll
    for (int n = 0; n < 8; ++n) {
      f32x4 oacc = (f32x4){0.f, 0.f, 0.f, 0.f};
#pragma unroll
      for (int ks = 0; ks < 2; ++ks) {
        bf16x8 vf;
#pragma unroll
        for (int j = 0; j < 8; ++j) vf[j] = (short)V[(ks * 32 + (lane >> 4) * 8 + j) * 136 + n * 16 + (lane & 15)];
        oacc = __builtin_amdgcn_mfma_f32_16x16x32_bf16(pf[ks], vf, oacc, 0, 0, 0);
      }
#pragma unroll
      for (int j = 0; j < 4; ++j)
        o[(size_t)(seg * 32 + mt * 16 + (lane >> 4) * 4 + j) * B_HD + h * 128 + n * 16 + (lane & 15)] = f2bf(oacc[j]);
    }
  }
}

constexpr unsigned kSpinCap = 1u << 22;

__device__ __forceinline__ bool spin_fail(unsigned& spins, gu32* tmo, int lane) {
  __builtin_amdgcn_s_sleep(1);
  if (++spins > kSpinCap) {
    if (lane == 0) __hip_atomic_store(tmo, 1u, RLX_AGENT);
    return true;
  }
  return (spins & 255u) == 0u && __builtin_amdgcn_readfirstlane(__hip_atomic_load(tmo, RLX_AGENT));
}
__device__ __forceinline__ void store_granule(gu64* g, unsigned epoch, unsigned value) {
  __hip_atomic_store(g, ((unsigned long long)epoch << 32) | value, RLX_AGENT);
}
template <int N>
__device__ __forceinline__ bool sweep_granules(gu64* g, int S, unsigned epoch, unsigned (&v)[N], gu32* tmo, int lane) {
  for (unsigned spins = 0;;) {
    bool ok = true;
#pragma unroll
    for (int k = 0; k < N; ++k) {
      const unsigned long long x = __hip_atomic_load(g + k * S, RLX_AGENT);
      v[k] = (unsigned)x;
      ok &= (x >> 32) == epoch;
    }
    if (__all(ok)) return true;
    if (spin_fail(spins, tmo, lane)) return false;
  }
}

__device__ __forceinline__ u32x4 load16_sc1(__amdgpu_buffer_rsrc_t rs, unsigned byte_off) {
  return __builtin_amdgcn_raw_buffer_load_b128(rs, byte_off, 0, 16);
}
struct ChainArgs {
  const u16* mem0;
  const u16* Wq;
  const u16* Wk;
  const u16* Wv;
  const u16* Wo;
  const u16* qkvB;
  u16* kvm;
  unsigned long long* gMem;
  unsigned long long* gQK;
  unsigned long long* gO;
  unsigned* tmo;
};

#define STAMP(idx) do {} while (0)
constexpr int MEM_LDS_STRIDE = 1032;

__global__ __launch_bounds__(256, 1) void chain_kernel(ChainArgs a) {
  extern __shared__ __attribute__((aligned(16))) char lds[];
  const int b = blockIdx.x;
  const int tid = threadIdx.x, lane = tid & 63;
  const int w = __builtin_amdgcn_readfirstlane(tid >> 6);
  gu64* gMem = (gu64*)a.gMem;
  gu64* gQK = (gu64*)a.gQK;
  gu64* gO = (gu64*)a.gO;
  gu32* tmo = (gu32*)a.tmo;
  const __amdgpu_buffer_rsrc_t rsMem = __builtin_amdgcn_make_buffer_rsrc((void*)a.gMem, 0, 32768, 0x00020000);
  const __amdgpu_buffer_rsrc_t rsO = __builtin_amdgcn_make_buffer_rsrc((void*)a.gO, 0, 131072, 0x00020000);

  if (b < 192) {
    const int t = b / 64, cb = b % 64;
    const u16* W = t == 0 ? a.Wq : (t == 1 ? a.Wk : a.Wv);
    const int col = cb * 64 + w * 16 + (lane & 15);
    bf16x8 wf[32];
#pragma unroll
    for (int i = 0; i < 32; ++i) {
#pragma unroll
      for (int j = 0; j < 8; ++j) wf[i][j] = (short)W[(size_t)(i * 32 + (lane >> 4) * 8 + j) * B_HD + col];
      asm volatile("" : "+v"(wf[i]));
    }
    u16* sMem = (u16*)lds;
    u16* sQ = (u16*)(lds + 16896);
    u16* sK = sQ + 8 * 136;
    u16* sVT = sK + 48 * 136;
    if (t == 2) {
      for (int i = tid; i < 8 * 136; i += 256) sK[40 * 136 + i] = 0;
      for (int i = tid; i < 64 * 8; i += 256) sVT[(i >> 3) * 56 + 40 + (i & 7)] = 0;
    }
    const int h = cb >> 1, half = cb & 1;
    for (int s = 0; s < B_NSEG; ++s) {
      const unsigned epoch = (unsigned)s + 1u;
      u32x4 kxr0 = (u32x4){0, 0, 0, 0}, kxr1 = (u32x4){0, 0, 0, 0}, vxr = (u32x4){0, 0, 0, 0};
      if (t == 2) {
        {
          int r = tid >> 4, ch = tid & 15;
          kxr0 = *reinterpret_cast<const u32x4*>(a.qkvB + (size_t)(s * 32 + r) * QKVB_LD + B_HD + h * 128 + ch * 8);
          kxr1 = *reinterpret_cast<const u32x4*>(a.qkvB + (size_t)(s * 32 + 16 + r) * QKVB_LD + B_HD + h * 128 + ch * 8);
        }
        {
          int r = tid >> 3, ch = tid & 7;
          vxr = *reinterpret_cast<const u32x4*>(a.qkvB + (size_t)(s * 32 + r) * QKVB_LD + 2 * B_HD + h * 128 +
                                                half * 64 + ch * 8);
        }
      }
      STAMP(0);
      if (s == 0) {
#pragma unroll
        for (int k = 0; k < 16; ++k) {
          int g = tid + 256 * k;
          int r = g >> 9, cp = g & 511;
          unsigned v = *reinterpret_cast<const unsigned*>(a.mem0 + r * 1024 + cp * 2);
          *reinterpret_cast<unsigned*>(sMem + r * MEM_LDS_STRIDE + cp * 2) = v;
        }
      } else {
        if (w == 0) {
          unsigned spins = 0;
          for (;;) {
            const unsigned long long x = __hip_atomic_load(gMem + 7 * 512 + lane * 8 + 7, RLX_AGENT);
            if (__all((x >> 32) == (unsigned)s)) break;
            if (spin_fail(spins, tmo, lane)) break;
          }
        }
        __syncthreads();
        {
          u32x4 x[8];
          unsigned spins = 0;
          for (;;) {
#pragma unroll
            for (int k = 0; k < 8; ++k) x[k] = load16_sc1(rsMem, 16u * (tid + 256 * k));
            bool ok = true;
#pragma unroll
            for (int k = 0; k < 8; ++k) ok &= (x[k][1] == (unsigned)s) & (x[k][3] == (unsigned)s);
            if (__all(ok)) break;
            if (spin_fail(spins, tmo, lane)) return;
          }
#pragma unroll
          for (int k = 0; k < 8; ++k) {
            int g = 2 * (tid + 256 * k);
            int r = g >> 9, cp = g & 511;
            *reinterpret_cast<unsigned*>(sMem + r * MEM_LDS_STRIDE + cp * 2) = x[k][0];
            *reinterpret_cast<unsigned*>(sMem + r * MEM_LDS_STRIDE + cp * 2 + 2) = x[k][2];
          }
        }
      }
      __syncthreads();
      STAMP(1);
      f32x4 acc = (f32x4){0.f, 0.f, 0.f, 0.f}, acc2 = (f32x4){0.f, 0.f, 0.f, 0.f};
      {
        bf16x8 af[32];
#pragma unroll
        for (int i = 0; i < 32; ++i)
          af[i] = *reinterpret_cast<const bf16x8*>(sMem + (lane & 7) * MEM_LDS_STRIDE + i * 32 + (lane >> 4) * 8);
#pragma unroll
        for (int i = 0; i < 32; i += 2) {
          acc = __builtin_amdgcn_mfma_f32_16x16x32_bf16(wf[i], af[i], acc, 0, 0, 0);
          acc2 = __builtin_amdgcn_mfma_f32_16x16x32_bf16(wf[i + 1], af[i + 1], acc2, 0, 0, 0);
        }
        acc += acc2;
      }
      {
        if ((lane & 15) < 8) {
          const int r = lane & 15;
#pragma unroll
          for (int jj = 0; jj < 2; ++jj) {
            const unsigned pk = (unsigned)f2bf(acc[2 * jj]) | ((unsigned)f2bf(acc[2 * jj + 1]) << 16);
            const int cp = w * 8 + (lane >> 4) * 2 + jj;
            if (t < 2) store_granule(gQK + (size_t)t * 16384 + r * 2048 + cb * 32 + cp, epoch, pk);
            if (t >= 1)
              *reinterpret_cast<unsigned*>(a.kvm + ((size_t)(s * 2 + (t - 1)) * 8 + r) * B_HD + cb * 64 + cp * 2) = pk;
            if (t == 2) {
              sVT[(cp * 2) * 56 + r] = (u16)(pk & 0xffff);
              sVT[(cp * 2 + 1) * 56 + r] = (u16)(pk >> 16);
            }
          }
        }
      }
      STAMP(2);
      if (t == 2) {
        {
          int r = tid >> 4, ch = tid & 15;
          *reinterpret_cast<u32x4*>(sK + (8 + r) * 136 + ch * 8) = kxr0;
          *reinterpret_cast<u32x4*>(sK + (24 + r) * 136 + ch * 8) = kxr1;
        }
        {
          int r = tid >> 3, ch = tid & 7;
#pragma unroll
          for (int e = 0; e < 4; ++e) {
            sVT[(ch * 8 + 2 * e) * 56 + 8 + r] = (u16)(vxr[e] & 0xffff);
            sVT[(ch * 8 + 2 * e + 1) * 56 + 8 + r] = (u16)(vxr[e] >> 16);
          }
        }
        {
          unsigned v[4];
          int r = tid >> 5, cp = tid & 31;
          {
            unsigned spins = 0;
            for (;;) {
              bool ok = true;
#pragma unroll
              for (int k = 0; k < 4; ++k) {
                const unsigned long long x = __hip_atomic_load(
                    gQK + (size_t)(k >> 1) * 16384 + r * 2048 + h * 64 + (k & 1) * 32 + cp, RLX_AGENT);
                v[k] = (unsigned)x;
                ok &= (x >> 32) == epoch;
              }
              if (__all(ok)) break;
              if (spin_fail(spins, tmo, lane)) return;
            }
          }
          *reinterpret_cast<unsigned*>(sQ + r * 136 + cp * 2) = v[0];
          *reinterpret_cast<unsigned*>(sQ + r * 136 + 64 + cp * 2) = v[1];
          *reinterpret_cast<unsigned*>(sK + r * 136 + cp * 2) = v[2];
          *reinterpret_cast<unsigned*>(sK + r * 136 + 64 + cp * 2) = v[3];
        }
        __syncthreads();
        STAMP(3);
        if (w == 0) {
          bf16x8 qf[4], kf[3][4];
#pragma unroll
          for (int ks = 0; ks < 4; ++ks)
            qf[ks] = *reinterpret_cast<const bf16x8*>(sQ + (lane & 7) * 136 + ks * 32 + (lane >> 4) * 8);
#pragma unroll
          for (int tt = 0; tt < 3; ++tt)
#pragma unroll
            for (int ks = 0; ks < 4; ++ks)
              kf[tt][ks] = *reinterpret_cast<const bf16x8*>(sK + (tt * 16 + (lane & 15)) * 136 + ks * 32 + (lane >> 4) * 8);
          bf16x4 vlo[4], vmid[4], vhi[4];
#pragma unroll
          for (int n = 0; n < 4; ++n) {
            const u16* vp = sVT + (n * 16 + (lane & 15)) * 56 + (lane >> 4) * 4;
            vlo[n] = *reinterpret_cast<const bf16x4*>(vp);
            vmid[n] = *reinterpret_cast<const bf16x4*>(vp + 16);
            vhi[n] = *reinterpret_cast<const bf16x4*>(vp + 32);
          }
          f32x4 sc[3];
          float mx = -3e38f;
#pragma unroll
          for (int tt = 0; tt < 3; ++tt) {
            f32x4 sv = (f32x4){0.f, 0.f, 0.f, 0.f};
#pragma unroll
            for (int ks = 0; ks < 4; ++ks) sv = __builtin_amdgcn_mfma_f32_16x16x32_bf16(kf[tt][ks], qf[ks], sv, 0, 0, 0);
#pragma unroll
            for (int j = 0; j < 4; ++j) {
              const int key = tt * 16 + (lane >> 4) * 4 + j;
              float v = rbf(rbf(sv[j]) * 0.08838834764831845f);
              v = key < 40 ? v : -3e38f;
              sv[j] = v;
              mx = fmaxf(mx, v);
            }
            sc[tt] = sv;
          }
          mx = fmaxf(mx, __shfl_xor(mx, 16));
          mx = fmaxf(mx, __shfl_xor(mx, 32));
          float sum = 0.f;
#pragma unroll
          for (int tt = 0; tt < 3; ++tt)
#pragma unroll
            for (int j = 0; j < 4; ++j) {
              float e = __expf(sc[tt][j] - mx);
              sc[tt][j] = e;
              sum += e;
            }
          sum += __shfl_xor(sum, 16);
          sum += __shfl_xor(sum, 32);
          const float inv = 1.f / sum;
          bf16x8 p0, p1;
#pragma unroll
          for (int j = 0; j < 4; ++j) {
            p0[j] = (short)f2bf(sc[0][j] * inv);
            p0[4 + j] = (short)f2bf(sc[1][j] * inv);
            p1[j] = (short)f2bf(sc[2][j] * inv);
            p1[4 + j] = 0;
          }
#pragma unroll
          for (int n = 0; n < 4; ++n) {
            bf16x8 va = (bf16x8){vlo[n][0], vlo[n][1], vlo[n][2], vlo[n][3], vmid[n][0], vmid[n][1], vmid[n][2], vmid[n][3]};
            bf16x8 vb = (bf16x8){vhi[n][0], vhi[n][1], vhi[n][2], vhi[n][3], 0, 0, 0, 0};
            f32x4 oacc = __builtin_amdgcn_mfma_f32_16x16x32_bf16(va, p0, (f32x4){0.f, 0.f, 0.f, 0.f}, 0, 0, 0);
            oacc = __builtin_amdgcn_mfma_f32_16x16x32_bf16(vb, p1, oacc, 0, 0, 0);
            if ((lane & 15) < 8) {
#pragma unroll
              for (int jj = 0; jj < 2; ++jj)
                store_granule(gO + (lane & 15) * 2048 + cb * 32 + n * 8 + (lane >> 4) * 2 + jj, epoch,
                              (unsigned)f2bf(oacc[2 * jj]) | ((unsigned)f2bf(oacc[2 * jj + 1]) << 16));
            }
          }
          STAMP(4);
        }
      }
      __syncthreads();
    }
  } else {
    const int ob = b - 192;
    const int col = ob * 16 + (lane & 15);
    bf16x8 wf[32];
#pragma unroll
    for (int i = 0; i < 32; ++i) {
#pragma unroll
      for (int j = 0; j < 8; ++j)
        wf[i][j] = (short)a.Wo[(size_t)(w * 1024 + i * 32 + (lane >> 4) * 8 + j) * D_MODEL + col];
      asm volatile("" : "+v"(wf[i]));
    }
    u16* sO = (u16*)lds + w * (8 * 1032);
    float* sRed = (float*)(lds + 4 * 8 * 1032 * 2);
    for (int s = 0; s < B_NSEG; ++s) {
      const unsigned epoch = (unsigned)s + 1u;
      STAMP(0);
      {
        unsigned spins = 0;
        for (;;) {
          const unsigned long long x = __hip_atomic_load(gO + 7 * 2048 + w * 512 + (lane & 15) * 32 + 31, RLX_AGENT);
          bool ok = (x >> 32) == epoch;
          if (__all(ok)) break;
          if (spin_fail(spins, tmo, lane)) return;
        }
      }
      {
        u32x4 x[32];
        unsigned spins = 0;
        for (;;) {
#pragma unroll
          for (int k = 0; k < 32; ++k) x[k] = load16_sc1(rsO, 8u * ((k >> 2) * 2048 + w * 512 + ((k & 3) * 64 + lane) * 2));
          bool ok = true;
#pragma unroll
          for (int k = 0; k < 32; ++k) ok &= (x[k][1] == epoch) & (x[k][3] == epoch);
          if (__all(ok)) break;
          if (spin_fail(spins, tmo, lane)) return;
        }
#pragma unroll
        for (int k = 0; k < 32; ++k) {
          u16* d = sO + (k >> 2) * 1032 + ((k & 3) * 64 + lane) * 4;
          *reinterpret_cast<unsigned*>(d) = x[k][0];
          *reinterpret_cast<unsigned*>(d + 2) = x[k][2];
        }
      }
      __builtin_amdgcn_s_waitcnt(0xc07f);
      STAMP(1);
      f32x4 acc = (f32x4){0.f, 0.f, 0.f, 0.f}, acc2 = (f32x4){0.f, 0.f, 0.f, 0.f};
      {
        bf16x8 af[32];
#pragma unroll
        for (int i = 0; i < 32; ++i)
          af[i] = *reinterpret_cast<const bf16x8*>(sO + (lane & 7) * 1032 + i * 32 + (lane >> 4) * 8);
#pragma unroll
        for (int i = 0; i < 32; i += 2) {
          acc = __builtin_amdgcn_mfma_f32_16x16x32_bf16(wf[i], af[i], acc, 0, 0, 0);
          acc2 = __builtin_amdgcn_mfma_f32_16x16x32_bf16(wf[i + 1], af[i + 1], acc2, 0, 0, 0);
        }
        acc += acc2;
      }
      if ((lane & 15) < 8) {
#pragma unroll
        for (int j = 0; j < 4; ++j) sRed[(w * 8 + (lane & 15)) * 16 + (lane >> 4) * 4 + j] = acc[j];
      }
      __syncthreads();
      if (tid < 64) {
        int r = tid >> 3, cp = tid & 7;
        float v0 = 0.f, v1 = 0.f;
#pragma unroll
        for (int q = 0; q < 4; ++q) {
          v0 += sRed[(q * 8 + r) * 16 + cp * 2];
          v1 += sRed[(q * 8 + r) * 16 + cp * 2 + 1];
        }
        unsigned pk = (unsigned)f2bf(v0) | ((unsigned)f2bf(v1) << 16);
        if (s + 1 < B_NSEG) store_granule(gMem + r * 512 + ob * 8 + cp, epoch, pk);
      }
      STAMP(2);
      __syncthreads();
    }
  }
}

extern "C" void kernel_launch(void* const* d_in, const int* in_sizes, int n_in, void* d_out, int out_size, void* d_ws,
                              size_t ws_size, hipStream_t stream) {
  const u16* A_x = (const u16*)d_in[0];
  const u16* A_wqkv = (const u16*)d_in[1];
  const u16* A_wo = (const u16*)d_in[2];
  const u16* B_x = (const u16*)d_in[3];
  const u16* B_mem0 = (const u16*)d_in[4];
  const u16* B_Wq = (const u16*)d_in[5];
  const u16* B_Wk = (const u16*)d_in[6];
  const u16* B_Wv = (const u16*)d_in[7];
  const u16* B_Wo = (const u16*)d_in[8];
  u16* out = (u16*)d_out;
  char* ws = (char*)d_ws;
  u16* qkvB = (u16*)ws;
  u16* qkvA = (u16*)(ws + (size_t)50331648);
  u16* oA = (u16*)(ws + (size_t)50331648 + 12582912);
  u16* kvm = (u16*)(ws + (size_t)67108864);
  u16* oB = (u16*)(ws + (size_t)75497472);
  char* gran = ws + (size_t)92274688;
  const size_t gran_bytes = 32768 + 262144 + 131072 + 256;
  (void)hipMemsetAsync(gran, 0, gran_bytes, stream);

  gemm128<<<(2048 / 128) * (4096 / 128), 256, 0, stream>>>(B_x, 1024, B_Wq, 4096, qkvB, QKVB_LD, 2048, 4096, 1024);
  gemm128<<<(2048 / 128) * (4096 / 128), 256, 0, stream>>>(B_x, 1024, B_Wk, 4096, qkvB + 4096, QKVB_LD, 2048, 4096, 1024);
  gemm128<<<(2048 / 128) * (4096 / 128), 256, 0, stream>>>(B_x, 1024, B_Wv, 4096, qkvB + 8192, QKVB_LD, 2048, 4096, 1024);
  ChainArgs ca;
  ca.mem0 = B_mem0;
  ca.Wq = B_Wq;
  ca.Wk = B_Wk;
  ca.Wv = B_Wv;
  ca.Wo = B_Wo;
  ca.qkvB = qkvB;
  ca.kvm = kvm;
  ca.gMem = (unsigned long long*)gran;
  ca.gQK = (unsigned long long*)(gran + 32768);
  ca.gO = (unsigned long long*)(gran + 32768 + 262144);
  ca.tmo = (unsigned*)(gran + 32768 + 262144 + 131072);
  (void)hipFuncSetAttribute((const void*)chain_kernel, hipFuncAttributeMaxDynamicSharedMemorySize, 96 * 1024);
  chain_kernel<<<256, 256, 96 * 1024, stream>>>(ca);
  attnBx<<<B_NSEG * 8, 256, 0, stream>>>(qkvB, kvm, oB);
  gemm128<<<(2048 / 128) * (1024 / 128), 256, 0, stream>>>(oB, 4096, B_Wo, 1024, out + 1024, OUT_LD, 2048, 1024, 4096);
  gemm128<<<(2048 / 128) * (3072 / 128), 256, 0, stream>>>(A_x, 1024, A_wqkv, 3072, qkvA, QKVA_LD, 2048, 3072, 1024);
  attnA<<<(2048 / 64) * A_H, 256, 0, stream>>>(qkvA, oA);
  gemm128<<<(2048 / 128) * (1024 / 128), 256, 0, stream>>>(oA, 1024, A_wo, 1024, out, OUT_LD, 2048, 1024, 1024);
}
```
